```python
import torch

def _softmax(x, axis=-1):
    return torch.softmax(x, dim=axis)

def _gelu(x):
    return torch.nn.functional.gelu(x, approximate='tanh')

def _silu(x):
    return torch.nn.functional.silu(x)

def _sigmoid(x):
    return torch.sigmoid(x)

def _layernorm(x, axis=-1, eps=1e-05):
    m = x.mean(dim=axis, keepdim=True)
    v = x.var(dim=axis, keepdim=True, unbiased=False)
    return (x - m) / torch.sqrt(v + eps)

def _scatter_add(tgt, idx, src, axis=0):
    return tgt.clone().index_add_(axis, idx, src)

def _segment_sum(src, seg, n):
    out = torch.zeros((n,) + src.shape[1:], dtype=src.dtype)
    return out.index_add_(0, seg, src)

def _pad(x, pw):
    flat = tuple((v for pair in reversed(pw) for v in pair))
    return torch.nn.functional.pad(x, flat)
kernel_name = 'adaLN_cond__ssm_conv_s4__glu_gated_swiglu__prompt'

def setup_inputs():

    _w = lambda a: (lambda t: t.to(torch.bfloat16) if t.is_floating_point() else t)(torch.as_tensor(a))

    def _A_setup():
        B, L, D, N = (1, 2048, 640, 128)
        A_re = -(lambda _x: torch.exp(torch.as_tensor(_x)))((lambda *s: torch.randn(*s, dtype=torch.bfloat16) * 3.2)(D, N))
        A_im = torch.pi * (lambda *s: torch.randn(*s, dtype=torch.bfloat16) * 0.05)(D, N)
        B_re = (lambda *s: torch.randn(*s, dtype=torch.bfloat16) * 0.1)(D, N)
        B_im = (lambda *s: torch.randn(*s, dtype=torch.bfloat16) * 0.1)(D, N)
        C_re = (lambda *s: torch.randn(*s, dtype=torch.bfloat16) * 0.1)(D, N)
        C_im = (lambda *s: torch.randn(*s, dtype=torch.bfloat16) * 0.1)(D, N)
        log_dt = (lambda *s: torch.randn(*s, dtype=torch.bfloat16) * 0.05)(D) * 0.5 - 4.0
        Dskip = (lambda *s: torch.randn(*s, dtype=torch.bfloat16) * 0.05)(D)
        return {'u': _w((lambda *s: torch.randn(*s, dtype=torch.bfloat16) * 0.05)(B, L, D)), 'A_re': _w(A_re), 'A_im': _w(A_im), 'B_re': _w(B_re), 'B_im': _w(B_im), 'C_re': _w(C_re), 'C_im': _w(C_im), 'log_dt': _w(log_dt), 'Dskip': _w(Dskip)}
    _A_in = _A_setup()

    def _B_setup():
        B, S, D, F = (1, 2048, 640, 1728)
        return {'x': _w((lambda *s: torch.randn(*s, dtype=torch.bfloat16) * 0.05)(B, S, D)), 'w1': _w((lambda *s: torch.randn(*s, dtype=torch.bfloat16) * 0.4)(D, F) * (2.0 / D) ** 0.5), 'w3': _w((lambda *s: torch.randn(*s, dtype=torch.bfloat16) * 0.4)(D, F) * (2.0 / D) ** 0.5), 'w2': _w((lambda *s: torch.randn(*s, dtype=torch.bfloat16) * 0.4)(F, D) * (2.0 / F) ** 0.5)}
    _B_in = _B_setup()
    return {'A_u': _A_in['u'], 'A_A_re': _A_in['A_re'], 'A_A_im': _A_in['A_im'], 'A_B_re': _A_in['B_re'], 'A_B_im': _A_in['B_im'], 'A_C_re': _A_in['C_re'], 'A_C_im': _A_in['C_im'], 'A_log_dt': _A_in['log_dt'], 'A_Dskip': _A_in['Dskip'], 'B_x': _B_in['x'], 'B_w1': _B_in['w1'], 'B_w3': _B_in['w3'], 'B_w2': _B_in['w2'], 'c': _w((lambda *s: torch.randn(*s, dtype=torch.bfloat16) * 0.2)(1, 640) * 8.0), 'w_mod': _w((lambda *s: torch.randn(*s, dtype=torch.bfloat16) * 0.2)(640, 6 * 640) * 640 ** (-0.5) * 8.0)}

def reference(A_u, A_A_re, A_A_im, A_B_re, A_B_im, A_C_re, A_C_im, A_log_dt, A_Dskip, B_x, B_w1, B_w3, B_w2, c, w_mod):

    def _A_ref(u, A_re, A_im, B_re, B_im, C_re, C_im, log_dt, Dskip):
        Bsz, L, D = u.shape
        dt = torch.exp(log_dt)[:, None]
        A = A_re + 1j * A_im
        Bc = B_re + 1j * B_im
        Cc = C_re + 1j * C_im
        logdA = dt * A
        dB = dt * Bc
        l = (lambda x, dt: x.to(dt))(torch.arange(L), A_re.dtype)
        P = torch.exp(logdA[:, :, None] * l[None, None, :])
        K = torch.einsum('dn,dnl,dn->dl', Cc, P, dB).real
        L2 = 2 * L
        Kp = _pad(K, ((0, 0), (0, L)))
        up = (lambda x, *d: x.permute(*d))(u, 0, 2, 1)
        up = _pad(up, ((0, 0), (0, 0), (0, L)))
        Kf = (lambda x, axes=(-2, -1): torch.fft.rfft2(x.float(), dim=axes))(Kp[:, None, :], axes=(-2, -1))
        Uf = (lambda x, axes=(-2, -1): torch.fft.rfft2(x.float(), dim=axes))(up[:, :, None, :], axes=(-2, -1))
        Yf = Uf * Kf[None, :, :, :]
        y = (lambda x, s, axes=(-2, -1): torch.fft.irfft2(x, s=s, dim=axes).to(torch.bfloat16))(Yf, (1, L2), axes=(-2, -1))[:, :, 0, :L]
        y = (lambda x, *d: x.permute(*d))(y, 0, 2, 1) + u * Dskip[None, None, :]
        return y

    def _B_ref(x, w1, w3, w2):
        gate = torch.einsum('bsd,df->bsf', x, w1)
        up = torch.einsum('bsd,df->bsf', x, w3)
        h = _silu(gate) * up
        out = torch.einsum('bsf,fd->bsd', h, w2)
        return out
    x = A_u
    mod = (_silu(c) @ w_mod).reshape(c.shape[0], 6, 1, -1)
    sa, ca, ga, sb, cb, gb = (mod[:, 0].clone(), mod[:, 1].clone(), mod[:, 2].clone(), mod[:, 3].clone(), mod[:, 4].clone(), mod[:, 5].clone())
    h = _layernorm(x, axis=-1) * (1 + ca) + sa
    x = x + ga * _A_ref(h, A_A_re, A_A_im, A_B_re, A_B_im, A_C_re, A_C_im, A_log_dt, A_Dskip)
    h = _layernorm(x, axis=-1) * (1 + cb) + sb
    x = x + gb * _B_ref(h, B_w1, B_w3, B_w2)
    return x
```

```cpp
#include <hip/hip_runtime.h>
#include <hip/hip_bf16.h>

typedef __attribute__((ext_vector_type(8))) short bf16x8;
typedef __attribute__((ext_vector_type(4))) float f32x4;
typedef __attribute__((ext_vector_type(4))) unsigned u32x4;
typedef __attribute__((ext_vector_type(2))) unsigned u32x2;
typedef unsigned short u16;

#define DI __device__ __forceinline__

constexpr int L_ = 2048, D_ = 640, N_ = 128, F_ = 1728;

constexpr size_t OFF_MOD = 0;
constexpr size_t OFF_K0 = 16384;
constexpr size_t OFF_XNT = 32768;
constexpr size_t SZ_ACT = (size_t)D_ * L_ * 2;
constexpr size_t OFF_XT = OFF_XNT + SZ_ACT;
constexpr size_t OFF_KR = OFF_XT + SZ_ACT;
constexpr size_t OFF_X1T = OFF_KR + 2 * SZ_ACT;
constexpr size_t OFF_X1 = OFF_X1T + SZ_ACT;
constexpr size_t OFF_H2F = OFF_X1 + SZ_ACT;
constexpr size_t OFF_W13F = OFF_H2F + SZ_ACT;
constexpr size_t SZ_W = (size_t)D_ * F_ * 2;
constexpr size_t OFF_W2F = OFF_W13F + 2 * SZ_W;
constexpr size_t OFF_HIDF = OFF_W2F + SZ_W;
constexpr size_t WS_TOTAL = OFF_HIDF + (size_t)L_ * F_ * 2;

DI float bf2f(unsigned v) { return __uint_as_float(v << 16); }
DI float bflo(unsigned v) { return __uint_as_float(v << 16); }
DI float bfhi(unsigned v) { return __uint_as_float(v & 0xffff0000u); }
DI float rbf(float x) { return (float)(__bf16)x; }
DI unsigned f2bf(float x) { __bf16 b = (__bf16)x; return (unsigned)__builtin_bit_cast(u16, b); }
DI unsigned pack2(float lo, float hi) { return f2bf(lo) | (f2bf(hi) << 16); }

DI void transpose8x8(const u32x4* in, u32x4* out) {
#pragma unroll
  for (int d = 0; d < 4; ++d) {
#pragma unroll
    for (int p = 0; p < 4; ++p) {
      unsigned a = in[2 * p][d], b = in[2 * p + 1][d];
      out[2 * d][p] = (a & 0xffffu) | (b << 16);
      out[2 * d + 1][p] = (a >> 16) | (b & 0xffff0000u);
    }
  }
}

DI void cexp_l(float ar, float air, float l, float& er, float& ei) {
  float mag = __expf(ar * l);
  float th = air * l;
  float rev = th * 0.15915494309189535f;
  rev = rev - floorf(rev);
  er = mag * __builtin_amdgcn_cosf(rev);
  ei = mag * __builtin_amdgcn_sinf(rev);
}

constexpr int K1_KGEN = 640, K1_LN = 128, K1_MOD = 60, K1_RT = 203;

__global__ __launch_bounds__(256) void k1_prep(
    const u16* __restrict__ x, const u16* __restrict__ A_re, const u16* __restrict__ A_im,
    const u16* __restrict__ B_re, const u16* __restrict__ B_im, const u16* __restrict__ C_re,
    const u16* __restrict__ C_im, const u16* __restrict__ log_dt, const u16* __restrict__ w1,
    const u16* __restrict__ w3, const u16* __restrict__ w2, const u16* __restrict__ cvec,
    const u16* __restrict__ w_mod, char* __restrict__ ws) {
  __shared__ __attribute__((aligned(16))) float sm[4 * 4 * 256 + 704];
  const int t = threadIdx.x;
  int bid = blockIdx.x;
  if (bid < K1_KGEN) {
    const int d = bid;
    const int w = t >> 6, lane = t & 63, i = lane & 15, kk = lane >> 4;
    const float dt = rbf(__expf(bf2f(log_dt[d])));
    f32x4 acc0 = {0, 0, 0, 0}, acc1 = {0, 0, 0, 0}, acc2 = {0, 0, 0, 0}, acc3 = {0, 0, 0, 0};
    const float fi = (float)i;
#pragma unroll 2
    for (int s = 0; s < 8; ++s) {
      const int n = d * N_ + 32 * w + 4 * s + kk;
      const float ar = dt * bf2f(A_re[n]), ai = dt * bf2f(A_im[n]);
      const float dbr = dt * bf2f(B_re[n]), dbi = dt * bf2f(B_im[n]);
      const float cr0 = bf2f(C_re[n]), ci0 = bf2f(C_im[n]);
      const float cr = cr0 * dbr - ci0 * dbi, ci = cr0 * dbi + ci0 * dbr;
      float er, ei, xr, xi, pr, pi;
      cexp_l(ar, ai, fi, er, ei);
      cexp_l(ar, ai, 16.f * fi, xr, xi);
      pr = xr * cr - xi * ci; pi = xr * ci + xi * cr;
      acc0 = __builtin_amdgcn_mfma_f32_16x16x4f32(pr, er, acc0, 0, 0, 0);
      acc0 = __builtin_amdgcn_mfma_f32_16x16x4f32(-pi, ei, acc0, 0, 0, 0);
      cexp_l(ar, ai, 2.f * fi, er, ei);
      cexp_l(ar, ai, 256.f + 32.f * fi, xr, xi);
      pr = xr * cr - xi * ci; pi = xr * ci + xi * cr;
      acc1 = __builtin_amdgcn_mfma_f32_16x16x4f32(pr, er, acc1, 0, 0, 0);
      acc1 = __builtin_amdgcn_mfma_f32_16x16x4f32(-pi, ei, acc1, 0, 0, 0);
      cexp_l(ar, ai, 4.f * fi, er, ei);
      cexp_l(ar, ai, 512.f + 64.f * fi, xr, xi);
      pr = xr * cr - xi * ci; pi = xr * ci + xi * cr;
      acc2 = __builtin_amdgcn_mfma_f32_16x16x4f32(pr, er, acc2, 0, 0, 0);
      acc2 = __builtin_amdgcn_mfma_f32_16x16x4f32(-pi, ei, acc2, 0, 0, 0);
      cexp_l(ar, ai, 8.f * fi, er, ei);
      cexp_l(ar, ai, 1024.f + 128.f * fi, xr, xi);
      pr = xr * cr - xi * ci; pi = xr * ci + xi * cr;
      acc3 = __builtin_amdgcn_mfma_f32_16x16x4f32(pr, er, acc3, 0, 0, 0);
      acc3 = __builtin_amdgcn_mfma_f32_16x16x4f32(-pi, ei, acc3, 0, 0, 0);
    }
    float* red = sm;
#pragma unroll
    for (int j = 0; j < 4; ++j) {
      const int idx = 16 * (4 * kk + j) + i;
      red[(w * 4 + 0) * 256 + idx] = acc0[j];
      red[(w * 4 + 1) * 256 + idx] = acc1[j];
      red[(w * 4 + 2) * 256 + idx] = acc2[j];
      red[(w * 4 + 3) * 256 + idx] = acc3[j];
    }
    __syncthreads();
    float* Kd = sm + 4096;
    for (int ci = t; ci < 641; ci += 256) {
      int tile, idx;
      if (ci < 256) { tile = 0; idx = ci; }
      else if (ci < 384) { tile = 1; idx = ci - 256; }
      else if (ci < 512) { tile = 2; idx = ci - 384; }
      else { tile = 3; idx = ci - 512; }
      float v = red[(0 * 4 + tile) * 256 + idx] + red[(1 * 4 + tile) * 256 + idx] +
                red[(2 * 4 + tile) * 256 + idx] + red[(3 * 4 + tile) * 256 + idx];
      Kd[ci] = v;
    }
    __syncthreads();
    if (t == 0) ((float*)(ws + OFF_K0))[d] = Kd[0];
    u32x4 vh, vl;
#pragma unroll
    for (int e2 = 0; e2 < 4; ++e2) {
      unsigned ph[2], pl[2];
#pragma unroll
      for (int u = 0; u < 2; ++u) {
        const int ii = 8 * t + 2 * e2 + u;
        const int lag = 2048 - ii;
        float kv = 0.f;
        if (lag < 2048) {
          const int r = (int)rbf((float)lag);
          int ci;
          if (r < 256) ci = r;
          else if (r < 512) ci = 256 + ((r - 256) >> 1);
          else if (r < 1024) ci = 384 + ((r - 512) >> 2);
          else ci = 512 + ((r - 1024) >> 3);
          kv = Kd[ci];
        }
        const float hi = rbf(kv);
        ph[u] = f2bf(hi);
        pl[u] = f2bf(kv - hi);
      }
      vh[e2] = ph[0] | (ph[1] << 16);
      vl[e2] = pl[0] | (pl[1] << 16);
    }
    u32x4* kr = (u32x4*)(ws + OFF_KR + (size_t)d * 2 * 2048 * 2);
    kr[t] = vh;
    kr[256 + t] = vl;
    return;
  }
  bid -= K1_KGEN;
  if (bid < K1_LN) {
    const int l0 = bid * 16;
    const bool act = t < 160;
    const int rg = t / 80, ch = t % 80;
    u32x4 v[8];
    float* red = sm;
    float* stat = sm + 1280;
    if (act) {
#pragma unroll
      for (int i = 0; i < 8; ++i) {
        v[i] = *(const u32x4*)(x + (size_t)(l0 + 8 * rg + i) * D_ + 8 * ch);
        float s = 0.f;
#pragma unroll
        for (int e = 0; e < 4; ++e) s += bflo(v[i][e]) + bfhi(v[i][e]);
        red[(8 * rg + i) * 80 + ch] = s;
      }
    }
    __syncthreads();
    {
      const int row = t >> 4, part = t & 15;
      float s = 0.f;
#pragma unroll
      for (int e = 0; e < 5; ++e) s += red[row * 80 + part * 5 + e];
#pragma unroll
      for (int o = 1; o < 16; o <<= 1) s += __shfl_xor(s, o);
      if (part == 0) stat[row] = s * (1.f / 640.f);
    }
    __syncthreads();
    float mean[8];
    if (act) {
#pragma unroll
      for (int i = 0; i < 8; ++i) {
        mean[i] = stat[8 * rg + i];
        float s = 0.f;
#pragma unroll
        for (int e = 0; e < 4; ++e) {
          float a = bflo(v[i][e]) - mean[i], b = bfhi(v[i][e]) - mean[i];
          s += a * a + b * b;
        }
        red[(8 * rg + i) * 80 + ch] = s;
      }
    }
    __syncthreads();
    {
      const int row = t >> 4, part = t & 15;
      float s = 0.f;
#pragma unroll
      for (int e = 0; e < 5; ++e) s += red[row * 80 + part * 5 + e];
#pragma unroll
      for (int o = 1; o < 16; o <<= 1) s += __shfl_xor(s, o);
      if (part == 0) stat[16 + row] = s * (1.f / 640.f);
    }
    __syncthreads();
    if (act) {
      u32x4 nv[8];
#pragma unroll
      for (int i = 0; i < 8; ++i) {
        const float mb = rbf(mean[i]);
        const float den = rbf(sqrtf(rbf(rbf(stat[16 + 8 * rg + i]) + 1e-5f)));
#pragma unroll
        for (int e = 0; e < 4; ++e) {
          float a = rbf(bflo(v[i][e]) - mb) / den, b = rbf(bfhi(v[i][e]) - mb) / den;
          nv[i][e] = pack2(a, b);
        }
      }
      u32x4 o[8];
      transpose8x8(nv, o);
      u16* xnT = (u16*)(ws + OFF_XNT);
#pragma unroll
      for (int c = 0; c < 8; ++c) *(u32x4*)(xnT + (size_t)(8 * ch + c) * L_ + l0 + 8 * rg) = o[c];
      transpose8x8(v, o);
      u16* xT = (u16*)(ws + OFF_XT);
#pragma unroll
      for (int c = 0; c < 8; ++c) *(u32x4*)(xT + (size_t)(8 * ch + c) * L_ + l0 + 8 * rg) = o[c];
    }
    return;
  }
  bid -= K1_LN;
  if (bid < K1_MOD) {
    const int j0 = bid * 64;
    const int cc = t & 7, rgp = t >> 3;
    float acc[8];
#pragma unroll
    for (int e = 0; e < 8; ++e) acc[e] = 0.f;
#pragma unroll 4
    for (int r = 0; r < 20; ++r) {
      const int i = rgp * 20 + r;
      const float cv = bf2f(cvec[i]);
      const float sv = rbf(cv / (1.f + __expf(-cv)));
      const u32x4 wv = *(const u32x4*)(w_mod + (size_t)i * 3840 + j0 + 8 * cc);
#pragma unroll
      for (int e = 0; e < 4; ++e) {
        acc[2 * e] += sv * bflo(wv[e]);
        acc[2 * e + 1] += sv * bfhi(wv[e]);
      }
    }
    float* red = sm;
#pragma unroll
    for (int e = 0; e < 8; ++e) red[rgp * 64 + 8 * cc + e] = acc[e];
    __syncthreads();
    if (t < 64) {
      float s = 0.f;
#pragma unroll 8
      for (int r = 0; r < 32; ++r) s += red[r * 64 + t];
      ((float*)(ws + OFF_MOD))[j0 + t] = rbf(s);
    }
    return;
  }
  bid -= K1_MOD;
  {
    const int task = bid * 256 + t;
    if (task >= 3 * 17280) return;
    const int which = task / 17280, tk = task % 17280;
    u32x4 v[8], o[8];
    if (which < 2) {
      const int nc = tk % 216, kc = tk / 216;
      const u16* src = which == 0 ? w1 : w3;
#pragma unroll
      for (int i = 0; i < 8; ++i) v[i] = *(const u32x4*)(src + (size_t)(8 * kc + i) * F_ + 8 * nc);
      transpose8x8(v, o);
      const int nt = nc >> 1, kt = kc >> 2, g = kc & 3;
      char* dst = ws + OFF_W13F + ((size_t)((nt * 20 + kt) * 2 + which)) * 1024 + (size_t)(16 * g + 8 * (nc & 1)) * 16;
#pragma unroll
      for (int c = 0; c < 8; ++c) *(u32x4*)(dst + c * 16) = o[c];
    } else {
      const int nc = tk % 80, kc = tk / 80;
#pragma unroll
      for (int i = 0; i < 8; ++i) v[i] = *(const u32x4*)(w2 + (size_t)(8 * kc + i) * D_ + 8 * nc);
      transpose8x8(v, o);
      const int nt = nc >> 1, kt = kc >> 2, g = kc & 3;
      char* dst = ws + OFF_W2F + ((size_t)(nt * 54 + kt)) * 1024 + (size_t)(16 * g + 8 * (nc & 1)) * 16;
#pragma unroll
      for (int c = 0; c < 8; ++c) *(u32x4*)(dst + c * 16) = o[c];
    }
  }
}

constexpr int UC_STRIDE = 4608;
constexpr int UC_BYTES = 8 * UC_STRIDE;
constexpr int KR_BYTES = 384 * 16;

__global__ __launch_bounds__(256) void k2_conv(const u16* __restrict__ xnT, const u16* __restrict__ xT,
                                               const u16* __restrict__ KR, const float* __restrict__ K0,
                                               const float* __restrict__ modv, const u16* __restrict__ Dskip,
                                               u16* __restrict__ x1T) {
  __shared__ __attribute__((aligned(16))) char sm[UC_BYTES + 2 * KR_BYTES];
  char* uc = sm;
  char* krh = sm + UC_BYTES;
  char* krl = krh + KR_BYTES;
  const int d = blockIdx.x, t = threadIdx.x;
  const int w = t >> 6, lane = t & 63, c = lane & 15, g = lane >> 4;

  const u32x4* xn4 = (const u32x4*)(xnT + (size_t)d * L_);
  u32x4 cur = xn4[t];
  u32x4 prv = {0, 0, 0, 0};
  if (t > 0) prv = xn4[t - 1];
  const u32x4* kr4 = (const u32x4*)(KR + (size_t)d * 2 * 2048);
  const u32x4 kh = kr4[t], kl = kr4[256 + t];
  const u32x4 xv = ((const u32x4*)(xT + (size_t)d * L_))[t];
  const float sa = modv[d], ca1 = rbf(1.f + modv[640 + d]), ga = modv[1280 + d];
  const float dsk = bf2f(Dskip[d]), k0 = K0[d];

  unsigned hv[8];
  float hf[8];
#pragma unroll
  for (int e = 0; e < 4; ++e) {
    float a = rbf(rbf(bflo(prv[e]) * ca1) + sa), b = rbf(rbf(bfhi(prv[e]) * ca1) + sa);
    hv[e] = (t > 0) ? pack2(a, b) : 0u;
    float a2 = rbf(rbf(bflo(cur[e]) * ca1) + sa), b2 = rbf(rbf(bfhi(cur[e]) * ca1) + sa);
    hf[2 * e] = a2; hf[2 * e + 1] = b2;
    hv[4 + e] = pack2(a2, b2);
  }
#pragma unroll
  for (int s = 0; s < 8; ++s) {
    u32x4 o;
    if ((s & 1) == 0) {
#pragma unroll
      for (int e = 0; e < 4; ++e) o[e] = hv[s / 2 + e];
    } else {
#pragma unroll
      for (int e = 0; e < 4; ++e) o[e] = (hv[s / 2 + e] >> 16) | (hv[s / 2 + e + 1] << 16);
    }
    *(u32x4*)(uc + s * UC_STRIDE + 64 * (s & 3) + 32 * (s >> 2) + 16 * (t + 7)) = o;
  }
  if (t < 56) {
    const int s = t / 7, m = t % 7;
    *(u32x4*)(uc + s * UC_STRIDE + 64 * (s & 3) + 32 * (s >> 2) + 16 * m) = u32x4{0, 0, 0, 0};
  }
  {
    const int ci = 8 + t, R = ci >> 3, e = ci & 7;
    const int pos = 8 * R + (e ^ (R & 7));
    *(u32x4*)(krh + 16 * pos) = kh;
    *(u32x4*)(krl + 16 * pos) = kl;
    if (t < 120) {
      *(u32x4*)(krh + 16 * (264 + t)) = u32x4{0, 0, 0, 0};
      *(u32x4*)(krl + 16 * (264 + t)) = u32x4{0, 0, 0, 0};
    }
  }
  __syncthreads();

  f32x4 acc[2][4];
#pragma unroll
  for (int a = 0; a < 2; ++a)
#pragma unroll
    for (int b = 0; b < 4; ++b) acc[a][b] = f32x4{0, 0, 0, 0};

  const int sg = c & 7;
  const char* ub = uc + sg * UC_STRIDE + 64 * (sg & 3) + 32 * (sg >> 2) + 16 * ((c >> 3) + g);
  const int k_lo = (w == 0) ? 0 : (w == 1) ? 12 : (w == 2) ? 24 : 40;
  const int k_hi = (w == 0) ? 12 : (w == 1) ? 24 : (w == 2) ? 40 : 64;
  const int kA_hi = k_hi < 32 ? k_hi : 32;
  for (int kp = k_lo; kp < kA_hi; ++kp) {
    bf16x8 bfr[4];
#pragma unroll
    for (int ct = 0; ct < 4; ++ct) bfr[ct] = *(const bf16x8*)(ub + 16 * (2 * ct + 4 * kp));
    const int e = 4 * (kp & 1) + g;
#pragma unroll
    for (int tt = 0; tt < 2; ++tt) {
      const int R = 32 - 16 * tt - c + (kp >> 1);
      const int pos = 8 * R + (e ^ (R & 7));
      const bf16x8 ah = *(const bf16x8*)(krh + 16 * pos);
      const bf16x8 al = *(const bf16x8*)(krl + 16 * pos);
#pragma unroll
      for (int ct = 0; ct < 4; ++ct) {
        acc[tt][ct] = __builtin_amdgcn_mfma_f32_16x16x32_bf16(ah, bfr[ct], acc[tt][ct], 0, 0, 0);
        acc[tt][ct] = __builtin_amdgcn_mfma_f32_16x16x32_bf16(al, bfr[ct], acc[tt][ct], 0, 0, 0);
      }
    }
  }
  for (int kp = (k_lo > 32 ? k_lo : 32); kp < k_hi; ++kp) {
    bf16x8 bfr[4];
#pragma unroll
    for (int ct = 0; ct < 4; ++ct) bfr[ct] = *(const bf16x8*)(ub + 16 * (2 * ct + 4 * kp));
    const int e = 4 * (kp & 1) + g;
    const int R = 16 - c + (kp >> 1);
    const int pos = 8 * R + (e ^ (R & 7));
    const bf16x8 ah = *(const bf16x8*)(krh + 16 * pos);
    const bf16x8 al = *(const bf16x8*)(krl + 16 * pos);
#pragma unroll
    for (int ct = 0; ct < 4; ++ct) {
      acc[1][ct] = __builtin_amdgcn_mfma_f32_16x16x32_bf16(ah, bfr[ct], acc[1][ct], 0, 0, 0);
      acc[1][ct] = __builtin_amdgcn_mfma_f32_16x16x32_bf16(al, bfr[ct], acc[1][ct], 0, 0, 0);
    }
  }
  __syncthreads();
  float* yp = (float*)sm + w * 2048;
#pragma unroll
  for (int tt = 0; tt < 2; ++tt)
#pragma unroll
    for (int ct = 0; ct < 4; ++ct)
#pragma unroll
      for (int j = 0; j < 4; ++j) yp[64 * (16 * tt + 4 * g + j) + 16 * ct + c] = acc[tt][ct][j];
  __syncthreads();
  const float* y0 = (const float*)sm + 8 * t;
  f32x4 s0 = *(const f32x4*)(y0), s1 = *(const f32x4*)(y0 + 4);
#pragma unroll
  for (int ww = 1; ww < 4; ++ww) {
    s0 += *(const f32x4*)(y0 + ww * 2048);
    s1 += *(const f32x4*)(y0 + ww * 2048 + 4);
  }
  float yv[8] = {s0[0], s0[1], s0[2], s0[3], s1[0], s1[1], s1[2], s1[3]};
  u32x4 ov;
#pragma unroll
  for (int e = 0; e < 4; ++e) {
    float r[2];
#pragma unroll
    for (int u = 0; u < 2; ++u) {
      const int i = 2 * e + u;
      const float yb = rbf(yv[i] + k0 * hf[i]);
      const float sk = rbf(hf[i] * dsk);
      const float sy = rbf(yb + sk);
      const float gy = rbf(ga * sy);
      const float xx = u ? bfhi(xv[e]) : bflo(xv[e]);
      r[u] = xx + gy;
    }
    ov[e] = pack2(r[0], r[1]);
  }
  ((u32x4*)(x1T + (size_t)d * L_))[t] = ov;
}

__global__ __launch_bounds__(320) void k3_ln2(const u16* __restrict__ x1T, const float* __restrict__ modv,
                                              u16* __restrict__ x1, char* __restrict__ h2F) {
  __shared__ float red[32 * 81];
  __shared__ float stat[64];
  const int t = threadIdx.x;
  const int l0 = blockIdx.x * 32;
  const int cg = t & 3, ch = t >> 2;
  u32x4 v[8];
#pragma unroll
  for (int i = 0; i < 8; ++i) v[i] = *(const u32x4*)(x1T + (size_t)(8 * ch + i) * L_ + l0 + 8 * cg);
  float sb[8], cb1[8];
#pragma unroll
  for (int i = 0; i < 8; ++i) {
    sb[i] = modv[3 * 640 + 8 * ch + i];
    cb1[i] = rbf(1.f + modv[4 * 640 + 8 * ch + i]);
  }
  u32x4 o[8];
  transpose8x8(v, o);
#pragma unroll
  for (int c = 0; c < 8; ++c) {
    float s = 0.f;
#pragma unroll
    for (int e = 0; e < 4; ++e) s += bflo(o[c][e]) + bfhi(o[c][e]);
    red[(8 * cg + c) * 81 + ch] = s;
  }
  __syncthreads();
  if (t < 256) {
    const int row = t >> 3, part = t & 7;
    float s = 0.f;
#pragma unroll
    for (int e = 0; e < 10; ++e) s += red[row * 81 + part * 10 + e];
#pragma unroll
    for (int of = 1; of < 8; of <<= 1) s += __shfl_xor(s, of);
    if (part == 0) stat[row] = s * (1.f / 640.f);
  }
  __syncthreads();
  float mean[8];
#pragma unroll
  for (int c = 0; c < 8; ++c) {
    mean[c] = stat[8 * cg + c];
    float s = 0.f;
#pragma unroll
    for (int e = 0; e < 4; ++e) {
      float a = bflo(o[c][e]) - mean[c], b = bfhi(o[c][e]) - mean[c];
      s += a * a + b * b;
    }
    red[(8 * cg + c) * 81 + ch] = s;
  }
  __syncthreads();
  if (t < 256) {
    const int row = t >> 3, part = t & 7;
    float s = 0.f;
#pragma unroll
    for (int e = 0; e < 10; ++e) s += red[row * 81 + part * 10 + e];
#pragma unroll
    for (int of = 1; of < 8; of <<= 1) s += __shfl_xor(s, of);
    if (part == 0) stat[32 + row] = s * (1.f / 640.f);
  }
  __syncthreads();
  const int kt = ch >> 2, gq = ch & 3;
#pragma unroll
  for (int c = 0; c < 8; ++c) {
    const int m = l0 + 8 * cg + c;
    const float mb = rbf(mean[c]);
    const float den = rbf(sqrtf(rbf(rbf(stat[32 + 8 * cg + c]) + 1e-5f)));
    u32x4 hv;
#pragma unroll
    for (int e = 0; e < 4; ++e) {
      float a = rbf(rbf(bflo(o[c][e]) - mb) / den), b = rbf(rbf(bfhi(o[c][e]) - mb) / den);
      a = rbf(a * cb1[2 * e]) + sb[2 * e];
      b = rbf(b * cb1[2 * e + 1]) + sb[2 * e + 1];
      hv[e] = pack2(a, b);
    }
    *(u32x4*)(h2F + ((size_t)((m >> 4) * 20 + kt)) * 1024 + (size_t)((m & 15) + 16 * gq) * 16) = hv;
    *(u32x4*)(x1 + (size_t)m * D_ + 8 * ch) = o[c];
  }
}

__global__ __launch_bounds__(256, 2) void k4_gemm1(const char* __restrict__ h2F, const char* __restrict__ W13F,
                                                   char* __restrict__ hidF) {
  __shared__ __attribute__((aligned(16))) char sm[2 * 32768];
  const int t = threadIdx.x, w = t >> 6, lane = t & 63;
  const int bn = blockIdx.x % 27, bm = blockIdx.x / 27;
  const int wm = w >> 1, wn = w & 1;
  auto stage = [&](int s, int buf) {
    char* base = sm + buf * 32768;
#pragma unroll
    for (int i = 0; i < 4; ++i) {
      const int f = w * 4 + i;
      const int mt = f >> 1, kt = f & 1;
      const char* src = h2F + ((size_t)((8 * bm + mt) * 20 + 2 * s + kt)) * 1024 + lane * 16;
      __builtin_amdgcn_global_load_lds((const unsigned*)src, (unsigned*)(base + f * 1024 + lane * 16), 16, 0, 0);
    }
#pragma unroll
    for (int i = 0; i < 4; ++i) {
      const int f = w * 4 + i;
      const char* src = W13F + ((size_t)((4 * bn + w) * 20 + 2 * s)) * 2048 + i * 1024 + lane * 16;
      __builtin_amdgcn_global_load_lds((const unsigned*)src, (unsigned*)(base + 16384 + f * 1024 + lane * 16), 16, 0, 0);
    }
  };
  f32x4 acc[2][2][4];
#pragma unroll
  for (int a = 0; a < 2; ++a)
#pragma unroll
    for (int b = 0; b < 2; ++b)
#pragma unroll
      for (int cI = 0; cI < 4; ++cI) acc[a][b][cI] = f32x4{0, 0, 0, 0};

  stage(0, 0);
  for (int s = 0; s < 10; ++s) {
    asm volatile("s_waitcnt vmcnt(0)" ::: "memory");
    __syncthreads();
    if (s + 1 < 10) stage(s + 1, (s + 1) & 1);
    const char* base = sm + (s & 1) * 32768;
#pragma unroll
    for (int kt = 0; kt < 2; ++kt) {
      bf16x8 hb[4], wa[2][2];
#pragma unroll
      for (int i = 0; i < 4; ++i) hb[i] = *(const bf16x8*)(base + ((4 * wm + i) * 2 + kt) * 1024 + lane * 16);
#pragma unroll
      for (int a = 0; a < 2; ++a)
#pragma unroll
        for (int b = 0; b < 2; ++b)
          wa[a][b] = *(const bf16x8*)(base + 16384 + (((2 * wn + a) * 2 + kt) * 2 + b) * 1024 + lane * 16);
#pragma unroll
      for (int a = 0; a < 2; ++a)
#pragma unroll
        for (int b = 0; b < 2; ++b)
#pragma unroll
          for (int i = 0; i < 4; ++i)
            acc[a][b][i] = __builtin_amdgcn_mfma_f32_16x16x32_bf16(wa[a][b], hb[i], acc[a][b][i], 0, 0, 0);
    }
  }
  const int c = lane & 15, g = lane >> 4;
#pragma unroll
  for (int a = 0; a < 2; ++a) {
    const int nt = 4 * bn + 2 * wn + a;
    const int kt2 = nt >> 1;
    const int slot = 16 * (2 * (nt & 1) + (g >> 1)) + c;
#pragma unroll
    for (int i = 0; i < 4; ++i) {
      const int mt = 8 * bm + 4 * wm + i;
      float hv[4];
#pragma unroll
      for (int j = 0; j < 4; ++j) {
        const float gv = rbf(acc[a][0][i][j]), uv = rbf(acc[a][1][i][j]);
        const float sv = rbf(gv / (1.f + __expf(-gv)));
        hv[j] = sv * uv;
      }
      u32x2 pv = {pack2(hv[0], hv[1]), pack2(hv[2], hv[3])};
      *(u32x2*)(hidF + ((size_t)(mt * 54 + kt2)) * 1024 + slot * 16 + (g & 1) * 8) = pv;
    }
  }
}

__global__ __launch_bounds__(256) void k5_gemm2(const char* __restrict__ hidF, const char* __restrict__ W2F,
                                                const u16* __restrict__ x1, const float* __restrict__ modv,
                                                u16* __restrict__ out) {
  __shared__ __attribute__((aligned(16))) float sm[4 * 20 * 256];
  const int t = threadIdx.x, w = t >> 6, lane = t & 63;
  const int bn = blockIdx.x & 7, bm = blockIdx.x >> 3;
  const int k_lo = (w == 0) ? 0 : (w == 1) ? 14 : (w == 2) ? 28 : 41;
  const int k_hi = (w == 0) ? 14 : (w == 1) ? 28 : (w == 2) ? 41 : 54;
  f32x4 acc[5][4];
#pragma unroll
  for (int a = 0; a < 5; ++a)
#pragma unroll
    for (int b = 0; b < 4; ++b) acc[a][b] = f32x4{0, 0, 0, 0};
  const char* hp = hidF + ((size_t)(4 * bm) * 54) * 1024 + lane * 16;
  const char* wp = W2F + ((size_t)(5 * bn) * 54) * 1024 + lane * 16;
  bf16x8 hb[4], wa[5], hbn[4], wan[5];
#pragma unroll
  for (int i = 0; i < 4; ++i) hb[i] = *(const bf16x8*)(hp + (size_t)(i * 54 + k_lo) * 1024);
#pragma unroll
  for (int i = 0; i < 5; ++i) wa[i] = *(const bf16x8*)(wp + (size_t)(i * 54 + k_lo) * 1024);
  for (int k = k_lo; k < k_hi; ++k) {
    const int kn = (k + 1 < k_hi) ? k + 1 : k;
#pragma unroll
    for (int i = 0; i < 4; ++i) hbn[i] = *(const bf16x8*)(hp + (size_t)(i * 54 + kn) * 1024);
#pragma unroll
    for (int i = 0; i < 5; ++i) wan[i] = *(const bf16x8*)(wp + (size_t)(i * 54 + kn) * 1024);
#pragma unroll
    for (int a = 0; a < 5; ++a)
#pragma unroll
      for (int b = 0; b < 4; ++b)
        acc[a][b] = __builtin_amdgcn_mfma_f32_16x16x32_bf16(wa[a], hb[b], acc[a][b], 0, 0, 0);
#pragma unroll
    for (int i = 0; i < 4; ++i) hb[i] = hbn[i];
#pragma unroll
    for (int i = 0; i < 5; ++i) wa[i] = wan[i];
  }
#pragma unroll
  for (int a = 0; a < 5; ++a)
#pragma unroll
    for (int b = 0; b < 4; ++b) *(f32x4*)(sm + ((w * 20 + a * 4 + b) * 64 + lane) * 4) = acc[a][b];
  __syncthreads();
  const int c = lane & 15, g = lane >> 4;
#pragma unroll
  for (int q = 0; q < 5; ++q) {
    const int tile = w * 5 + q;
    const int a = tile >> 2, b = tile & 3;
    f32x4 s = *(const f32x4*)(sm + ((0 * 20 + tile) * 64 + lane) * 4);
#pragma unroll
    for (int ww = 1; ww < 4; ++ww) s += *(const f32x4*)(sm + ((ww * 20 + tile) * 64 + lane) * 4);
    const int m = 16 * (4 * bm + b) + c;
    const int n = 16 * (5 * bn + a) + 4 * g;
    const u32x2 xv = *(const u32x2*)(x1 + (size_t)m * D_ + n);
    const f32x4 gb = *(const f32x4*)(modv + 5 * 640 + n);
    float r0 = bflo(xv[0]) + rbf(gb[0] * rbf(s[0]));
    float r1 = bfhi(xv[0]) + rbf(gb[1] * rbf(s[1]));
    float r2 = bflo(xv[1]) + rbf(gb[2] * rbf(s[2]));
    float r3 = bfhi(xv[1]) + rbf(gb[3] * rbf(s[3]));
    u32x2 ov = {pack2(r0, r1), pack2(r2, r3)};
    *(u32x2*)(out + (size_t)m * D_ + n) = ov;
  }
}

extern "C" void kernel_launch(void* const* d_in, const int* in_sizes, int n_in, void* d_out, int out_size,
                              void* d_ws, size_t ws_size, hipStream_t stream) {
  const u16* x = (const u16*)d_in[0];
  const u16* A_re = (const u16*)d_in[1];
  const u16* A_im = (const u16*)d_in[2];
  const u16* B_re = (const u16*)d_in[3];
  const u16* B_im = (const u16*)d_in[4];
  const u16* C_re = (const u16*)d_in[5];
  const u16* C_im = (const u16*)d_in[6];
  const u16* log_dt = (const u16*)d_in[7];
  const u16* Dskip = (const u16*)d_in[8];
  const u16* w1 = (const u16*)d_in[10];
  const u16* w3 = (const u16*)d_in[11];
  const u16* w2 = (const u16*)d_in[12];
  const u16* cvec = (const u16*)d_in[13];
  const u16* w_mod = (const u16*)d_in[14];
  char* ws = (char*)d_ws;
  u16* out = (u16*)d_out;

  k1_prep<<<K1_KGEN + K1_LN + K1_MOD + K1_RT, 256, 0, stream>>>(x, A_re, A_im, B_re, B_im, C_re, C_im, log_dt,
                                                                 w1, w3, w2, cvec, w_mod, ws);
  k2_conv<<<640, 256, 0, stream>>>((const u16*)(ws + OFF_XNT), (const u16*)(ws + OFF_XT), (const u16*)(ws + OFF_KR),
                                   (const float*)(ws + OFF_K0), (const float*)(ws + OFF_MOD), Dskip,
                                   (u16*)(ws + OFF_X1T));
  k3_ln2<<<64, 320, 0, stream>>>((const u16*)(ws + OFF_X1T), (const float*)(ws + OFF_MOD), (u16*)(ws + OFF_X1),
                                 ws + OFF_H2F);
  k4_gemm1<<<16 * 27, 256, 0, stream>>>(ws + OFF_H2F, ws + OFF_W13F, ws + OFF_HIDF);
  k5_gemm2<<<32 * 8, 256, 0, stream>>>(ws + OFF_HIDF, ws + OFF_W2F, (const u16*)(ws + OFF_X1),
                                       (const float*)(ws + OFF_MOD), out);
}
```

```cpp
#include <hip/hip_runtime.h>
#include <hip/hip_bf16.h>

typedef __attribute__((ext_vector_type(8))) short bf16x8;
typedef __attribute__((ext_vector_type(4))) float f32x4;
typedef __attribute__((ext_vector_type(4))) unsigned u32x4;
typedef __attribute__((ext_vector_type(2))) unsigned u32x2;
typedef unsigned short u16;

#define DI __device__ __forceinline__
#define STAMP(K, I)

constexpr int L_ = 2048, D_ = 640, N_ = 128, F_ = 1728;

constexpr size_t OFF_MOD = 0;
constexpr size_t OFF_K0 = 16384;
constexpr size_t OFF_XNT = 32768;
constexpr size_t SZ_ACT = (size_t)D_ * L_ * 2;
constexpr size_t OFF_XT = OFF_XNT + SZ_ACT;
constexpr size_t OFF_KR = OFF_XT + SZ_ACT;
constexpr size_t OFF_X1T = OFF_KR + 2 * SZ_ACT;
constexpr size_t OFF_X1 = OFF_X1T + SZ_ACT;
constexpr size_t OFF_H2F = OFF_X1 + SZ_ACT;
constexpr size_t OFF_W13F = OFF_H2F + SZ_ACT;
constexpr size_t SZ_W = (size_t)D_ * F_ * 2;
constexpr size_t OFF_W2F = OFF_W13F + 2 * SZ_W;
constexpr size_t OFF_HIDF = OFF_W2F + SZ_W;

DI float bf2f(unsigned v) { return __uint_as_float(v << 16); }
DI float bflo(unsigned v) { return __uint_as_float(v << 16); }
DI float bfhi(unsigned v) { return __uint_as_float(v & 0xffff0000u); }
DI float rbf(float x) { return (float)(__bf16)x; }
DI unsigned f2bf(float x) { __bf16 b = (__bf16)x; return (unsigned)__builtin_bit_cast(u16, b); }
DI unsigned pack2(float lo, float hi) { return f2bf(lo) | (f2bf(hi) << 16); }
DI float frcp(float x) { return __builtin_amdgcn_rcpf(x); }
DI float fdiv(float a, float b, float rb) { float q = a * rb; float r = fmaf(-q, b, a); return fmaf(r, rb, q); }

DI void transpose8x8(const u32x4* in, u32x4* out) {
#pragma unroll
  for (int d = 0; d < 4; ++d) {
#pragma unroll
    for (int p = 0; p < 4; ++p) {
      unsigned a = in[2 * p][d], b = in[2 * p + 1][d];
      out[2 * d][p] = (a & 0xffffu) | (b << 16);
      out[2 * d + 1][p] = (a >> 16) | (b & 0xffff0000u);
    }
  }
}

DI void cexp_l(float ar, float ai, float l, float& er, float& ei) {
  float mag = __expf(ar * l);
  float rev = (ai * l) * 0.15915494309189535f;
  rev = rev - floorf(rev);
  er = mag * __builtin_amdgcn_cosf(rev);
  ei = mag * __builtin_amdgcn_sinf(rev);
}
DI void csq(float& r, float& i) { float t = r * r - i * i; i = 2.f * r * i; r = t; }
DI void cmul(float ar, float ai, float br, float bi, float& r, float& i) { r = ar * br - ai * bi; i = ar * bi + ai * br; }

constexpr int K1_KGEN = 640, K1_LN = 256, K1_MOD = 120, K1_RT = 102;

__global__ __launch_bounds__(512) void k1_prep(
    const u16* __restrict__ x, const u16* __restrict__ A_re, const u16* __restrict__ A_im,
    const u16* __restrict__ B_re, const u16* __restrict__ B_im, const u16* __restrict__ C_re,
    const u16* __restrict__ C_im, const u16* __restrict__ log_dt, const u16* __restrict__ w1,
    const u16* __restrict__ w3, const u16* __restrict__ w2, const u16* __restrict__ cvec,
    const u16* __restrict__ w_mod, char* __restrict__ ws) {
  __shared__ __attribute__((aligned(16))) float sm[8 * 4 * 256 + 704];
  const int t = threadIdx.x;
  int bid = blockIdx.x;
  STAMP(0, 0);
  if (bid < K1_KGEN) {
    const int d = bid;
    const int w = t >> 6, lane = t & 63, i = lane & 15, kk = lane >> 4;
    u16 rA[4], rAi[4], rB[4], rBi[4], rC[4], rCi[4];
#pragma unroll
    for (int s = 0; s < 4; ++s) {
      const int n = d * N_ + 16 * w + 4 * s + kk;
      rA[s] = A_re[n]; rAi[s] = A_im[n]; rB[s] = B_re[n]; rBi[s] = B_im[n]; rC[s] = C_re[n]; rCi[s] = C_im[n];
    }
    const float dt = rbf(__expf(bf2f(log_dt[d])));
    f32x4 acc0 = {0, 0, 0, 0}, acc1 = {0, 0, 0, 0}, acc2 = {0, 0, 0, 0}, acc3 = {0, 0, 0, 0};
    const float fi = (float)i;
#pragma unroll
    for (int s = 0; s < 4; ++s) {
      const float ar = dt * bf2f(rA[s]), ai = dt * bf2f(rAi[s]);
      const float dbr = dt * bf2f(rB[s]), dbi = dt * bf2f(rBi[s]);
      const float cr0 = bf2f(rC[s]), ci0 = bf2f(rCi[s]);
      float cr, ci;
      cmul(cr0, ci0, dbr, dbi, cr, ci);
      float er, ei, xr, xi, gr, gi, pr, pi, qr, qi;
      cexp_l(ar, ai, fi, er, ei);
      cexp_l(ar, ai, 16.f * fi, xr, xi);
      cexp_l(ar, ai, 256.f, gr, gi);
      cmul(xr, xi, cr, ci, pr, pi);
      acc0 = __builtin_amdgcn_mfma_f32_16x16x4f32(pr, er, acc0, 0, 0, 0);
      acc0 = __builtin_amdgcn_mfma_f32_16x16x4f32(-pi, ei, acc0, 0, 0, 0);
      csq(er, ei); csq(xr, xi);
      cmul(gr, gi, cr, ci, qr, qi);
      cmul(xr, xi, qr, qi, pr, pi);
      acc1 = __builtin_amdgcn_mfma_f32_16x16x4f32(pr, er, acc1, 0, 0, 0);
      acc1 = __builtin_amdgcn_mfma_f32_16x16x4f32(-pi, ei, acc1, 0, 0, 0);
      csq(er, ei); csq(xr, xi); csq(gr, gi);
      cmul(gr, gi, cr, ci, qr, qi);
      cmul(xr, xi, qr, qi, pr, pi);
      acc2 = __builtin_amdgcn_mfma_f32_16x16x4f32(pr, er, acc2, 0, 0, 0);
      acc2 = __builtin_amdgcn_mfma_f32_16x16x4f32(-pi, ei, acc2, 0, 0, 0);
      csq(er, ei); csq(xr, xi); csq(gr, gi);
      cmul(gr, gi, cr, ci, qr, qi);
      cmul(xr, xi, qr, qi, pr, pi);
      acc3 = __builtin_amdgcn_mfma_f32_16x16x4f32(pr, er, acc3, 0, 0, 0);
      acc3 = __builtin_amdgcn_mfma_f32_16x16x4f32(-pi, ei, acc3, 0, 0, 0);
    }
    STAMP(0, 1);
    float* red = sm;
#pragma unroll
    for (int j = 0; j < 4; ++j) {
      const int idx = 16 * (4 * kk + j) + i;
      red[(w * 4 + 0) * 256 + idx] = acc0[j];
      red[(w * 4 + 1) * 256 + idx] = acc1[j];
      red[(w * 4 + 2) * 256 + idx] = acc2[j];
      red[(w * 4 + 3) * 256 + idx] = acc3[j];
    }
    __syncthreads();
    float* Kd = sm + 8192;
    for (int ci = t; ci < 641; ci += 512) {
      int tile, idx;
      if (ci < 256) { tile = 0; idx = ci; }
      else if (ci < 384) { tile = 1; idx = ci - 256; }
      else if (ci < 512) { tile = 2; idx = ci - 384; }
      else { tile = 3; idx = ci - 512; }
      float v = 0.f;
#pragma unroll
      for (int ww = 0; ww < 8; ++ww) v += red[(ww * 4 + tile) * 256 + idx];
      Kd[ci] = v;
    }
    __syncthreads();
    if (t == 0) ((float*)(ws + OFF_K0))[d] = Kd[0];
    unsigned ph[4], pl[4];
#pragma unroll
    for (int u = 0; u < 4; ++u) {
      const int ii = 4 * t + u;
      const int lag = 2048 - ii;
      float kv = 0.f;
      if (lag < 2048) {
        const int r = (int)rbf((float)lag);
        int ci;
        if (r < 256) ci = r;
        else if (r < 512) ci = 256 + ((r - 256) >> 1);
        else if (r < 1024) ci = 384 + ((r - 512) >> 2);
        else ci = 512 + ((r - 1024) >> 3);
        kv = Kd[ci];
      }
      const float hi = rbf(kv);
      ph[u] = f2bf(hi);
      pl[u] = f2bf(kv - hi);
    }
    u32x2* kr = (u32x2*)(ws + OFF_KR + (size_t)d * 2 * 2048 * 2);
    kr[t] = u32x2{ph[0] | (ph[1] << 16), ph[2] | (ph[3] << 16)};
    kr[512 + t] = u32x2{pl[0] | (pl[1] << 16), pl[2] | (pl[3] << 16)};
    STAMP(0, 2);
    return;
  }
  bid -= K1_KGEN;
  if (bid < K1_LN) {
    const int l0 = bid * 8;
    const int w = t >> 6, lane = t & 63;
    const unsigned* xr = (const unsigned*)(x + (size_t)(l0 + w) * D_);
    unsigned v[5];
#pragma unroll
    for (int j = 0; j < 5; ++j) v[j] = xr[lane + 64 * j];
    float s = 0.f;
#pragma unroll
    for (int j = 0; j < 5; ++j) s += bflo(v[j]) + bfhi(v[j]);
#pragma unroll
    for (int o = 1; o < 64; o <<= 1) s += __shfl_xor(s, o);
    const float mean = s * (1.f / 640.f);
    float q = 0.f;
#pragma unroll
    for (int j = 0; j < 5; ++j) {
      float a = bflo(v[j]) - mean, b = bfhi(v[j]) - mean;
      q += a * a + b * b;
    }
#pragma unroll
    for (int o = 1; o < 64; o <<= 1) q += __shfl_xor(q, o);
    const float mb = rbf(mean);
    const float den = rbf(sqrtf(rbf(rbf(q * (1.f / 640.f)) + 1e-5f)));
    const float rden = frcp(den);
    unsigned* sn = (unsigned*)sm;
    unsigned* sx = sn + 8 * 320;
#pragma unroll
    for (int j = 0; j < 5; ++j) {
      float a = fdiv(rbf(bflo(v[j]) - mb), den, rden), b = fdiv(rbf(bfhi(v[j]) - mb), den, rden);
      sn[w * 320 + lane + 64 * j] = pack2(a, b);
      sx[w * 320 + lane + 64 * j] = v[j];
    }
    __syncthreads();
    const u16* sn16 = (const u16*)sn;
    const u16* sx16 = (const u16*)sx;
    u16* xnT = (u16*)(ws + OFF_XNT);
    u16* xT = (u16*)(ws + OFF_XT);
    for (int dd = t; dd < 640; dd += 512) {
      u32x4 a, b;
#pragma unroll
      for (int e = 0; e < 4; ++e) {
        a[e] = (unsigned)sn16[(2 * e) * 640 + dd] | ((unsigned)sn16[(2 * e + 1) * 640 + dd] << 16);
        b[e] = (unsigned)sx16[(2 * e) * 640 + dd] | ((unsigned)sx16[(2 * e + 1) * 640 + dd] << 16);
      }
      *(u32x4*)(xnT + (size_t)dd * L_ + l0) = a;
      *(u32x4*)(xT + (size_t)dd * L_ + l0) = b;
    }
    STAMP(0, 2);
    return;
  }
  bid -= K1_LN;
  if (bid < K1_MOD) {
    const int j0 = bid * 32;
    const int cc = t & 3, rgp = t >> 2;
    u32x4 wv[5];
    u16 cv[5];
#pragma unroll
    for (int r = 0; r < 5; ++r) {
      const int i = rgp * 5 + r;
      wv[r] = *(const u32x4*)(w_mod + (size_t)i * 3840 + j0 + 8 * cc);
      cv[r] = cvec[i];
    }
    float acc[8];
#pragma unroll
    for (int e = 0; e < 8; ++e) acc[e] = 0.f;
#pragma unroll
    for (int r = 0; r < 5; ++r) {
      const float c_ = bf2f(cv[r]);
      const float sv = rbf(c_ / (1.f + __expf(-c_)));
#pragma unroll
      for (int e = 0; e < 4; ++e) {
        acc[2 * e] += sv * bflo(wv[r][e]);
        acc[2 * e + 1] += sv * bfhi(wv[r][e]);
      }
    }
    float* red = sm;
#pragma unroll
    for (int e = 0; e < 8; ++e) red[rgp * 33 + 8 * cc + e] = acc[e];
    __syncthreads();
    {
      const int col = t >> 4, part = t & 15;
      float s = 0.f;
#pragma unroll
      for (int r = 0; r < 8; ++r) s += red[(part * 8 + r) * 33 + col];
#pragma unroll
      for (int o = 1; o < 16; o <<= 1) s += __shfl_xor(s, o);
      if (part == 0) ((float*)(ws + OFF_MOD))[j0 + col] = rbf(s);
    }
    STAMP(0, 2);
    return;
  }
  bid -= K1_MOD;
  {
    const int task = bid * 512 + t;
    if (task >= 3 * 17280) return;
    const int which = task / 17280, tk = task % 17280;
    u32x4 v[8], o[8];
    if (which < 2) {
      const int nc = tk % 216, kc = tk / 216;
      const u16* src = which == 0 ? w1 : w3;
#pragma unroll
      for (int i = 0; i < 8; ++i) v[i] = *(const u32x4*)(src + (size_t)(8 * kc + i) * F_ + 8 * nc);
      transpose8x8(v, o);
      const int nt = nc >> 1, kt = kc >> 2, g = kc & 3;
      char* dst = ws + OFF_W13F + ((size_t)((nt * 20 + kt) * 2 + which)) * 1024 + (size_t)(16 * g + 8 * (nc & 1)) * 16;
#pragma unroll
      for (int c = 0; c < 8; ++c) *(u32x4*)(dst + c * 16) = o[c];
    } else {
      const int nc = tk % 80, kc = tk / 80;
#pragma unroll
      for (int i = 0; i < 8; ++i) v[i] = *(const u32x4*)(w2 + (size_t)(8 * kc + i) * D_ + 8 * nc);
      transpose8x8(v, o);
      const int nt = nc >> 1, kt = kc >> 2, g = kc & 3;
      char* dst = ws + OFF_W2F + ((size_t)(nt * 54 + kt)) * 1024 + (size_t)(16 * g + 8 * (nc & 1)) * 16;
#pragma unroll
      for (int c = 0; c < 8; ++c) *(u32x4*)(dst + c * 16) = o[c];
    }
    STAMP(0, 2);
  }
}

constexpr int UC_STRIDE = 4608;
constexpr int UC_BYTES = 8 * UC_STRIDE;
constexpr int KR_BYTES = 384 * 16;
constexpr int K2_LDS = 65536;

__global__ __launch_bounds__(512) void k2_conv(const u16* __restrict__ xnT, const u16* __restrict__ xT,
                                               const u16* __restrict__ KR, const float* __restrict__ K0,
                                               const float* __restrict__ modv, const u16* __restrict__ Dskip,
                                               u16* __restrict__ x1T) {
  __shared__ __attribute__((aligned(16))) char sm[K2_LDS];
  char* uc = sm;
  char* krh = sm + UC_BYTES;
  char* krl = krh + KR_BYTES;
  const int d = blockIdx.x, t = threadIdx.x;
  const int w = t >> 6, lane = t & 63, c = lane & 15, g = lane >> 4;
  STAMP(1, 0);
  const float sa = modv[d], ca1 = rbf(1.f + modv[640 + d]), ga = modv[1280 + d];
  const float dsk = bf2f(Dskip[d]), k0 = K0[d];
  const u32x2 xn_own = ((const u32x2*)(xnT + (size_t)d * L_))[t];
  const u32x2 x_own = ((const u32x2*)(xT + (size_t)d * L_))[t];

  if (t < 256) {
    const u32x4* xn4 = (const u32x4*)(xnT + (size_t)d * L_);
    u32x4 cur = xn4[t];
    u32x4 prv = {0, 0, 0, 0};
    if (t > 0) prv = xn4[t - 1];
    unsigned hv[8];
#pragma unroll
    for (int e = 0; e < 4; ++e) {
      float a = rbf(rbf(bflo(prv[e]) * ca1) + sa), b = rbf(rbf(bfhi(prv[e]) * ca1) + sa);
      hv[e] = (t > 0) ? pack2(a, b) : 0u;
      float a2 = rbf(rbf(bflo(cur[e]) * ca1) + sa), b2 = rbf(rbf(bfhi(cur[e]) * ca1) + sa);
      hv[4 + e] = pack2(a2, b2);
    }
#pragma unroll
    for (int s = 0; s < 8; ++s) {
      u32x4 o;
      if ((s & 1) == 0) {
#pragma unroll
        for (int e = 0; e < 4; ++e) o[e] = hv[s / 2 + e];
      } else {
#pragma unroll
        for (int e = 0; e < 4; ++e) o[e] = (hv[s / 2 + e] >> 16) | (hv[s / 2 + e + 1] << 16);
      }
      *(u32x4*)(uc + s * UC_STRIDE + 64 * (s & 3) + 32 * (s >> 2) + 16 * (t + 7)) = o;
    }
  } else {
    const int t2 = t - 256;
    const u32x4* kr4 = (const u32x4*)(KR + (size_t)d * 2 * 2048);
    const u32x4 kh = kr4[t2], kl = kr4[256 + t2];
    if (t2 < 56) {
      const int s = t2 / 7, m = t2 % 7;
      *(u32x4*)(uc + s * UC_STRIDE + 64 * (s & 3) + 32 * (s >> 2) + 16 * m) = u32x4{0, 0, 0, 0};
    }
    if (t2 < 120) {
      *(u32x4*)(krh + 16 * (264 + t2)) = u32x4{0, 0, 0, 0};
      *(u32x4*)(krl + 16 * (264 + t2)) = u32x4{0, 0, 0, 0};
    }
    const int ci = 8 + t2, R = ci >> 3, e = ci & 7;
    const int pos = 8 * R + (e ^ (R & 7));
    *(u32x4*)(krh + 16 * pos) = kh;
    *(u32x4*)(krl + 16 * pos) = kl;
  }
  __syncthreads();

  STAMP(1, 1);
  f32x4 acc[2][4];
#pragma unroll
  for (int a = 0; a < 2; ++a)
#pragma unroll
    for (int b = 0; b < 4; ++b) acc[a][b] = f32x4{0, 0, 0, 0};

  const int sg = c & 7;
  const char* ub = uc + sg * UC_STRIDE + 64 * (sg & 3) + 32 * (sg >> 2) + 16 * ((c >> 3) + g);
  int dA_lo, dA_hi, s_lo, s_hi;
  if (w < 5) { dA_lo = 6 * w; dA_hi = 6 * w + 6; s_lo = 32; s_hi = 32; }
  else if (w == 5) { dA_lo = 30; dA_hi = 32; s_lo = 32; s_hi = 40; }
  else { dA_lo = 32; dA_hi = 32; s_lo = 40 + 12 * (w - 6); s_hi = s_lo + 12; }
  for (int kp = dA_lo; kp < dA_hi; ++kp) {
    bf16x8 bfr[4];
#pragma unroll
    for (int ct = 0; ct < 4; ++ct) bfr[ct] = *(const bf16x8*)(ub + 16 * (2 * ct + 4 * kp));
    const int e = 4 * (kp & 1) + g;
    bf16x8 ah[2], al[2];
#pragma unroll
    for (int tt = 0; tt < 2; ++tt) {
      const int R = 32 - 16 * tt - c + (kp >> 1);
      const int pos = 8 * R + (e ^ (R & 7));
      ah[tt] = *(const bf16x8*)(krh + 16 * pos);
      al[tt] = *(const bf16x8*)(krl + 16 * pos);
    }
#pragma unroll
    for (int tt = 0; tt < 2; ++tt)
#pragma unroll
      for (int ct = 0; ct < 4; ++ct) {
        acc[tt][ct] = __builtin_amdgcn_mfma_f32_16x16x32_bf16(ah[tt], bfr[ct], acc[tt][ct], 0, 0, 0);
        acc[tt][ct] = __builtin_amdgcn_mfma_f32_16x16x32_bf16(al[tt], bfr[ct], acc[tt][ct], 0, 0, 0);
      }
  }
  for (int kp = s_lo; kp < s_hi; ++kp) {
    bf16x8 bfr[4];
#pragma unroll
    for (int ct = 0; ct < 4; ++ct) bfr[ct] = *(const bf16x8*)(ub + 16 * (2 * ct + 4 * kp));
    const int e = 4 * (kp & 1) + g;
    const int R = 16 - c + (kp >> 1);
    const int pos = 8 * R + (e ^ (R & 7));
    const bf16x8 ah = *(const bf16x8*)(krh + 16 * pos);
    const bf16x8 al = *(const bf16x8*)(krl + 16 * pos);
#pragma unroll
    for (int ct = 0; ct < 4; ++ct) {
      acc[1][ct] = __builtin_amdgcn_mfma_f32_16x16x32_bf16(ah, bfr[ct], acc[1][ct], 0, 0, 0);
      acc[1][ct] = __builtin_amdgcn_mfma_f32_16x16x32_bf16(al, bfr[ct], acc[1][ct], 0, 0, 0);
    }
  }
  __syncthreads();
  STAMP(1, 2);
  float* yp = (float*)sm + w * 2048;
#pragma unroll
  for (int tt = 0; tt < 2; ++tt)
#pragma unroll
    for (int ct = 0; ct < 4; ++ct)
#pragma unroll
      for (int j = 0; j < 4; ++j) yp[64 * (16 * tt + 4 * g + j) + 16 * ct + c] = acc[tt][ct][j];
  __syncthreads();
  const float* y0 = (const float*)sm + 4 * t;
  f32x4 s0 = *(const f32x4*)(y0);
#pragma unroll
  for (int ww = 1; ww < 8; ++ww) s0 += *(const f32x4*)(y0 + ww * 2048);
  float r[4];
#pragma unroll
  for (int i = 0; i < 4; ++i) {
    const float xn = (i & 1) ? bfhi(xn_own[i >> 1]) : bflo(xn_own[i >> 1]);
    const float hh = rbf(rbf(xn * ca1) + sa);
    const float yb = rbf(s0[i] + k0 * hh);
    const float sk = rbf(hh * dsk);
    const float sy = rbf(yb + sk);
    const float gy = rbf(ga * sy);
    const float xx = (i & 1) ? bfhi(x_own[i >> 1]) : bflo(x_own[i >> 1]);
    r[i] = xx + gy;
  }
  ((u32x2*)(x1T + (size_t)d * L_))[t] = u32x2{pack2(r[0], r[1]), pack2(r[2], r[3])};
  STAMP(1, 3);
}

__global__ __launch_bounds__(320) void k3_ln2(const u16* __restrict__ x1T, const float* __restrict__ modv,
                                              u16* __restrict__ x1, char* __restrict__ h2F) {
  __shared__ float red[16 * 81];
  __shared__ float stat[32];
  const int t = threadIdx.x;
  const int l0 = blockIdx.x * 16;
  STAMP(2, 0);
  const int cg = t & 3, ch = t >> 2;
  u32x2 v[8];
#pragma unroll
  for (int i = 0; i < 8; ++i) v[i] = *(const u32x2*)(x1T + (size_t)(8 * ch + i) * L_ + l0 + 4 * cg);
  const f32x4 sb0 = *(const f32x4*)(modv + 3 * 640 + 8 * ch), sb1 = *(const f32x4*)(modv + 3 * 640 + 8 * ch + 4);
  const f32x4 cb0 = *(const f32x4*)(modv + 4 * 640 + 8 * ch), cb1 = *(const f32x4*)(modv + 4 * 640 + 8 * ch + 4);
  u32x4 o[4];
#pragma unroll
  for (int dd = 0; dd < 2; ++dd)
#pragma unroll
    for (int p = 0; p < 4; ++p) {
      unsigned a = v[2 * p][dd], b = v[2 * p + 1][dd];
      o[2 * dd][p] = (a & 0xffffu) | (b << 16);
      o[2 * dd + 1][p] = (a >> 16) | (b & 0xffff0000u);
    }
#pragma unroll
  for (int c = 0; c < 4; ++c) {
    float s = 0.f;
#pragma unroll
    for (int e = 0; e < 4; ++e) s += bflo(o[c][e]) + bfhi(o[c][e]);
    red[(4 * cg + c) * 81 + ch] = s;
  }
  __syncthreads();
  if (t < 256) {
    const int row = t >> 4, part = t & 15;
    float s = 0.f;
#pragma unroll
    for (int e = 0; e < 5; ++e) s += red[row * 81 + part * 5 + e];
#pragma unroll
    for (int of = 1; of < 16; of <<= 1) s += __shfl_xor(s, of);
    if (part == 0) stat[row] = s * (1.f / 640.f);
  }
  __syncthreads();
  STAMP(2, 1);
  float mean[4];
#pragma unroll
  for (int c = 0; c < 4; ++c) {
    mean[c] = stat[4 * cg + c];
    float s = 0.f;
#pragma unroll
    for (int e = 0; e < 4; ++e) {
      float a = bflo(o[c][e]) - mean[c], b = bfhi(o[c][e]) - mean[c];
      s += a * a + b * b;
    }
    red[(4 * cg + c) * 81 + ch] = s;
  }
  __syncthreads();
  if (t < 256) {
    const int row = t >> 4, part = t & 15;
    float s = 0.f;
#pragma unroll
    for (int e = 0; e < 5; ++e) s += red[row * 81 + part * 5 + e];
#pragma unroll
    for (int of = 1; of < 16; of <<= 1) s += __shfl_xor(s, of);
    if (part == 0) stat[16 + row] = s * (1.f / 640.f);
  }
  __syncthreads();
  STAMP(2, 2);
  float sbv[8] = {sb0[0], sb0[1], sb0[2], sb0[3], sb1[0], sb1[1], sb1[2], sb1[3]};
  float cbv[8] = {cb0[0], cb0[1], cb0[2], cb0[3], cb1[0], cb1[1], cb1[2], cb1[3]};
#pragma unroll
  for (int i = 0; i < 8; ++i) cbv[i] = rbf(1.f + cbv[i]);
  const int kt = ch >> 2, gq = ch & 3;
#pragma unroll
  for (int c = 0; c < 4; ++c) {
    const int m = l0 + 4 * cg + c;
    const float mb = rbf(mean[c]);
    const float den = rbf(sqrtf(rbf(rbf(stat[16 + 4 * cg + c]) + 1e-5f)));
    const float rden = frcp(den);
    u32x4 hv;
#pragma unroll
    for (int e = 0; e < 4; ++e) {
      float a = rbf(fdiv(rbf(bflo(o[c][e]) - mb), den, rden)), b = rbf(fdiv(rbf(bfhi(o[c][e]) - mb), den, rden));
      a = rbf(a * cbv[2 * e]) + sbv[2 * e];
      b = rbf(b * cbv[2 * e + 1]) + sbv[2 * e + 1];
      hv[e] = pack2(a, b);
    }
    *(u32x4*)(h2F + ((size_t)((m >> 4) * 20 + kt)) * 1024 + (size_t)((m & 15) + 16 * gq) * 16) = hv;
    *(u32x4*)(x1 + (size_t)m * D_ + 8 * ch) = o[c];
  }
  STAMP(2, 3);
}

template <int N> DI void wait_vm() {
  if constexpr (N == 0) asm volatile("s_waitcnt vmcnt(0)" ::: "memory");
  else if constexpr (N == 3) asm volatile("s_waitcnt vmcnt(3)" ::: "memory");
  else if constexpr (N == 6) asm volatile("s_waitcnt vmcnt(6)" ::: "memory");
  else if constexpr (N == 9) asm volatile("s_waitcnt vmcnt(9)" ::: "memory");
  else if constexpr (N == 12) asm volatile("s_waitcnt vmcnt(12)" ::: "memory");
}

constexpr int G1_SLOT = 24576, G1_NSLOT = 6, G1_AHEAD = 5;

struct G1Ctx {
  const char* hsrc;
  const char* wsrc;
  char* sm;
  int w, lane;
};

DI void g1_issue(const G1Ctx& c, int s) {
  char* base = c.sm + (s % G1_NSLOT) * G1_SLOT;
  __builtin_amdgcn_global_load_lds((const unsigned*)(c.hsrc + (size_t)s * 1024),
                                   (unsigned*)(base + (2 * c.w) * 1024 + c.lane * 16), 16, 0, 0);
  __builtin_amdgcn_global_load_lds((const unsigned*)(c.hsrc + (size_t)s * 1024 + 20 * 1024),
                                   (unsigned*)(base + (2 * c.w + 1) * 1024 + c.lane * 16), 16, 0, 0);
  __builtin_amdgcn_global_load_lds((const unsigned*)(c.wsrc + (size_t)s * 2048),
                                   (unsigned*)(base + 16384 + c.w * 1024 + c.lane * 16), 16, 0, 0);
}

template <int S>
DI void g1_step(const G1Ctx& c, f32x4 (&acc)[2][2][4], int wm, int wn) {
  constexpr int inflight_after = (S + G1_AHEAD - 1 < 20 ? G1_AHEAD - 1 : 19 - S);
  wait_vm<3 * inflight_after>();
  asm volatile("s_waitcnt lgkmcnt(0)" ::: "memory");
  __builtin_amdgcn_s_barrier();
  asm volatile("" ::: "memory");
  if constexpr (S + G1_AHEAD < 20) g1_issue(c, S + G1_AHEAD);
  const char* base = c.sm + (S % G1_NSLOT) * G1_SLOT;
  bf16x8 hb[4], wa[2][2];
#pragma unroll
  for (int i = 0; i < 4; ++i) hb[i] = *(const bf16x8*)(base + (4 * wm + i) * 1024 + c.lane * 16);
#pragma unroll
  for (int a = 0; a < 2; ++a)
#pragma unroll
    for (int b = 0; b < 2; ++b)
      wa[a][b] = *(const bf16x8*)(base + 16384 + ((2 * wn + a) * 2 + b) * 1024 + c.lane * 16);
#pragma unroll
  for (int a = 0; a < 2; ++a)
#pragma unroll
    for (int b = 0; b < 2; ++b)
#pragma unroll
      for (int i = 0; i < 4; ++i)
        acc[a][b][i] = __builtin_amdgcn_mfma_f32_16x16x32_bf16(wa[a][b], hb[i], acc[a][b][i], 0, 0, 0);
  if constexpr (S + 1 < 20) g1_step<S + 1>(c, acc, wm, wn);
}

__global__ __launch_bounds__(512) void k4_gemm1(const char* __restrict__ h2F, const char* __restrict__ W13F,
                                                char* __restrict__ hidF) {
  __shared__ __attribute__((aligned(16))) char sm[G1_NSLOT * G1_SLOT];
  const int t = threadIdx.x, w = t >> 6, lane = t & 63;
  const int bn = blockIdx.x % 27, bm = blockIdx.x / 27;
  const int wm = w >> 1, wn = w & 1;
  STAMP(3, 0);
  G1Ctx c;
  c.sm = sm; c.w = w; c.lane = lane;
  c.hsrc = h2F + ((size_t)((16 * bm + 2 * w) * 20)) * 1024 + lane * 16;
  c.wsrc = W13F + ((size_t)((4 * bn + (w >> 1)) * 20)) * 2048 + (w & 1) * 1024 + lane * 16;
  f32x4 acc[2][2][4];
#pragma unroll
  for (int a = 0; a < 2; ++a)
#pragma unroll
    for (int b = 0; b < 2; ++b)
#pragma unroll
      for (int cI = 0; cI < 4; ++cI) acc[a][b][cI] = f32x4{0, 0, 0, 0};
#pragma unroll
  for (int s = 0; s < G1_AHEAD; ++s) g1_issue(c, s);
  g1_step<0>(c, acc, wm, wn);
  STAMP(3, 1);
  const int cl = lane & 15, g = lane >> 4;
#pragma unroll
  for (int a = 0; a < 2; ++a) {
    const int nt = 4 * bn + 2 * wn + a;
    const int kt2 = nt >> 1;
    const int slot = 16 * (2 * (nt & 1) + (g >> 1)) + cl;
#pragma unroll
    for (int i = 0; i < 4; ++i) {
      const int mt = 16 * bm + 4 * wm + i;
      float hv[4];
#pragma unroll
      for (int j = 0; j < 4; ++j) {
        const float gv = rbf(acc[a][0][i][j]), uv = rbf(acc[a][1][i][j]);
        const float dn = 1.f + __expf(-gv);
        const float sv = rbf(fdiv(gv, dn, frcp(dn)));
        hv[j] = sv * uv;
      }
      u32x2 pv = {pack2(hv[0], hv[1]), pack2(hv[2], hv[3])};
      *(u32x2*)(hidF + ((size_t)(mt * 54 + kt2)) * 1024 + slot * 16 + (g & 1) * 8) = pv;
    }
  }
  STAMP(3, 2);
}

__global__ __launch_bounds__(512) void k5_gemm2(const char* __restrict__ hidF, const char* __restrict__ W2F,
                                                const u16* __restrict__ x1, const float* __restrict__ modv,
                                                u16* __restrict__ out) {
  __shared__ __attribute__((aligned(16))) float sm[4 * 20 * 256];
  const int t = threadIdx.x, w = t >> 6, lane = t & 63;
  const int bn = blockIdx.x & 7, bm = blockIdx.x >> 3;
  const int k_lo = (w < 6) ? 7 * w : 42 + 6 * (w - 6);
  const int nk = (w < 6) ? 7 : 6;
  f32x4 acc[5][4];
#pragma unroll
  for (int a = 0; a < 5; ++a)
#pragma unroll
    for (int b = 0; b < 4; ++b) acc[a][b] = f32x4{0, 0, 0, 0};
  STAMP(4, 0);
  const char* hp = hidF + ((size_t)(4 * bm) * 54 + k_lo) * 1024 + lane * 16;
  const char* wp = W2F + ((size_t)(5 * bn) * 54 + k_lo) * 1024 + lane * 16;
  bf16x8 hb[3][4], wa[3][5];
#pragma unroll
  for (int p = 0; p < 2; ++p) {
#pragma unroll
    for (int i = 0; i < 4; ++i) hb[p][i] = *(const bf16x8*)(hp + (size_t)(i * 54 + p) * 1024);
#pragma unroll
    for (int i = 0; i < 5; ++i) wa[p][i] = *(const bf16x8*)(wp + (size_t)(i * 54 + p) * 1024);
  }
#pragma unroll
  for (int k = 0; k < 7; ++k) {
    if (k < nk) {
      if (k + 2 < nk) {
#pragma unroll
        for (int i = 0; i < 4; ++i) hb[(k + 2) % 3][i] = *(const bf16x8*)(hp + (size_t)(i * 54 + k + 2) * 1024);
#pragma unroll
        for (int i = 0; i < 5; ++i) wa[(k + 2) % 3][i] = *(const bf16x8*)(wp + (size_t)(i * 54 + k + 2) * 1024);
      }
#pragma unroll
      for (int a = 0; a < 5; ++a)
#pragma unroll
        for (int b = 0; b < 4; ++b)
          acc[a][b] = __builtin_amdgcn_mfma_f32_16x16x32_bf16(wa[k % 3][a], hb[k % 3][b], acc[a][b], 0, 0, 0);
    }
  }
  STAMP(4, 1);
  if (w >= 4) {
#pragma unroll
    for (int a = 0; a < 5; ++a)
#pragma unroll
      for (int b = 0; b < 4; ++b) *(f32x4*)(sm + (((w - 4) * 20 + a * 4 + b) * 64 + lane) * 4) = acc[a][b];
  }
  __syncthreads();
  if (w < 4) {
#pragma unroll
    for (int a = 0; a < 5; ++a)
#pragma unroll
      for (int b = 0; b < 4; ++b) acc[a][b] += *(const f32x4*)(sm + ((w * 20 + a * 4 + b) * 64 + lane) * 4);
  }
  __syncthreads();
  if (w < 4) {
#pragma unroll
    for (int a = 0; a < 5; ++a)
#pragma unroll
      for (int b = 0; b < 4; ++b) *(f32x4*)(sm + ((w * 20 + a * 4 + b) * 64 + lane) * 4) = acc[a][b];
  }
  __syncthreads();
  const int c = lane & 15, g = lane >> 4;
  STAMP(4, 2);
  const int t_lo = (w < 4) ? 3 * w : 12 + 2 * (w - 4);
  const int t_n = (w < 4) ? 3 : 2;
#pragma unroll
  for (int q = 0; q < 3; ++q) {
    if (q < t_n) {
      const int tile = t_lo + q;
      const int a = tile >> 2, b = tile & 3;
      const int m = 16 * (4 * bm + b) + c;
      const int n = 16 * (5 * bn + a) + 4 * g;
      const u32x2 xv = *(const u32x2*)(x1 + (size_t)m * D_ + n);
      const f32x4 gb = *(const f32x4*)(modv + 5 * 640 + n);
      f32x4 s = *(const f32x4*)(sm + ((0 * 20 + tile) * 64 + lane) * 4);
#pragma unroll
      for (int ww = 1; ww < 4; ++ww) s += *(const f32x4*)(sm + ((ww * 20 + tile) * 64 + lane) * 4);
      float r0 = bflo(xv[0]) + rbf(gb[0] * rbf(s[0]));
      float r1 = bfhi(xv[0]) + rbf(gb[1] * rbf(s[1]));
      float r2 = bflo(xv[1]) + rbf(gb[2] * rbf(s[2]));
      float r3 = bfhi(xv[1]) + rbf(gb[3] * rbf(s[3]));
      u32x2 ov = {pack2(r0, r1), pack2(r2, r3)};
      *(u32x2*)(out + (size_t)m * D_ + n) = ov;
    }
  }
  STAMP(4, 3);
}

extern "C" void kernel_launch(void* const* d_in, const int* in_sizes, int n_in, void* d_out, int out_size,
                              void* d_ws, size_t ws_size, hipStream_t stream) {
  const u16* x = (const u16*)d_in[0];
  const u16* A_re = (const u16*)d_in[1];
  const u16* A_im = (const u16*)d_in[2];
  const u16* B_re = (const u16*)d_in[3];
  const u16* B_im = (const u16*)d_in[4];
  const u16* C_re = (const u16*)d_in[5];
  const u16* C_im = (const u16*)d_in[6];
  const u16* log_dt = (const u16*)d_in[7];
  const u16* Dskip = (const u16*)d_in[8];
  const u16* w1 = (const u16*)d_in[10];
  const u16* w3 = (const u16*)d_in[11];
  const u16* w2 = (const u16*)d_in[12];
  const u16* cvec = (const u16*)d_in[13];
  const u16* w_mod = (const u16*)d_in[14];
  char* ws = (char*)d_ws;
  u16* out = (u16*)d_out;

  k1_prep<<<K1_KGEN + K1_LN + K1_MOD + K1_RT, 512, 0, stream>>>(x, A_re, A_im, B_re, B_im, C_re, C_im, log_dt,
                                                                 w1, w3, w2, cvec, w_mod, ws);
  k2_conv<<<640, 512, 0, stream>>>((const u16*)(ws + OFF_XNT), (const u16*)(ws + OFF_XT), (const u16*)(ws + OFF_KR),
                                   (const float*)(ws + OFF_K0), (const float*)(ws + OFF_MOD), Dskip,
                                   (u16*)(ws + OFF_X1T));
  k3_ln2<<<128, 320, 0, stream>>>((const u16*)(ws + OFF_X1T), (const float*)(ws + OFF_MOD), (u16*)(ws + OFF_X1),
                                  ws + OFF_H2F);
  k4_gemm1<<<8 * 27, 512, 0, stream>>>(ws + OFF_H2F, ws + OFF_W13F, ws + OFF_HIDF);
  k5_gemm2<<<32 * 8, 512, 0, stream>>>(ws + OFF_HIDF, ws + OFF_W2F, (const u16*)(ws + OFF_X1),
                                       (const float*)(ws + OFF_MOD), out);
}
```

```cpp
#include <hip/hip_runtime.h>
#include <hip/hip_bf16.h>

typedef __attribute__((ext_vector_type(8))) short bf16x8;
typedef __attribute__((ext_vector_type(4))) float f32x4;
typedef __attribute__((ext_vector_type(4))) unsigned u32x4;
typedef __attribute__((ext_vector_type(2))) unsigned u32x2;
typedef unsigned short u16;

#define DI __device__ __forceinline__
#define STAMP(K, I)

constexpr int L_ = 2048, D_ = 640, N_ = 128, F_ = 1728;

constexpr size_t OFF_MOD = 0;
constexpr size_t OFF_K0 = 16384;
constexpr size_t OFF_XNT = 32768;
constexpr size_t SZ_ACT = (size_t)D_ * L_ * 2;
constexpr int KD_STRIDE = 648;
constexpr size_t OFF_KD = OFF_XNT + SZ_ACT;
constexpr size_t OFF_GYT = OFF_KD + (size_t)D_ * KD_STRIDE * 4;
constexpr size_t OFF_X1 = OFF_GYT + SZ_ACT;
constexpr size_t OFF_H2F = OFF_X1 + SZ_ACT;
constexpr size_t OFF_W13F = OFF_H2F + SZ_ACT;
constexpr size_t SZ_W = (size_t)D_ * F_ * 2;
constexpr size_t OFF_W2F = OFF_W13F + 2 * SZ_W;
constexpr size_t OFF_HIDF = OFF_W2F + SZ_W;

DI float bf2f(unsigned v) { return __uint_as_float(v << 16); }
DI float bflo(unsigned v) { return __uint_as_float(v << 16); }
DI float bfhi(unsigned v) { return __uint_as_float(v & 0xffff0000u); }
DI float rbf(float x) { return (float)(__bf16)x; }
DI unsigned f2bf(float x) { __bf16 b = (__bf16)x; return (unsigned)__builtin_bit_cast(u16, b); }
DI unsigned pack2(float lo, float hi) { return f2bf(lo) | (f2bf(hi) << 16); }
DI float frcp(float x) { return __builtin_amdgcn_rcpf(x); }
DI float fdiv(float a, float b, float rb) { float q = a * rb; float r = fmaf(-q, b, a); return fmaf(r, rb, q); }

DI void transpose8x8(const u32x4* in, u32x4* out) {
#pragma unroll
  for (int d = 0; d < 4; ++d) {
#pragma unroll
    for (int p = 0; p < 4; ++p) {
      unsigned a = in[2 * p][d], b = in[2 * p + 1][d];
      out[2 * d][p] = (a & 0xffffu) | (b << 16);
      out[2 * d + 1][p] = (a >> 16) | (b & 0xffff0000u);
    }
  }
}

DI void cexp_l(float ar, float ai, float l, float& er, float& ei) {
  float mag = __expf(ar * l);
  float rev = (ai * l) * 0.15915494309189535f;
  rev = rev - floorf(rev);
  er = mag * __builtin_amdgcn_cosf(rev);
  ei = mag * __builtin_amdgcn_sinf(rev);
}
DI void csq(float& r, float& i) { float t = r * r - i * i; i = 2.f * r * i; r = t; }
DI void cmul(float ar, float ai, float br, float bi, float& r, float& i) { r = ar * br - ai * bi; i = ar * bi + ai * br; }

constexpr int K1_KGEN = 640, K1_LN = 256, K1_MOD = 120, K1_RT = 102;

__global__ __launch_bounds__(512) void k1_prep(
    const u16* __restrict__ x, const u16* __restrict__ A_re, const u16* __restrict__ A_im,
    const u16* __restrict__ B_re, const u16* __restrict__ B_im, const u16* __restrict__ C_re,
    const u16* __restrict__ C_im, const u16* __restrict__ log_dt, const u16* __restrict__ w1,
    const u16* __restrict__ w3, const u16* __restrict__ w2, const u16* __restrict__ cvec,
    const u16* __restrict__ w_mod, char* __restrict__ ws) {
  __shared__ __attribute__((aligned(16))) float sm[8 * 4 * 256 + 704];
  const int t = threadIdx.x;
  int bid = blockIdx.x;
  STAMP(0, 0);
  if (bid < K1_KGEN) {
    const int d = bid;
    const int w = t >> 6, lane = t & 63, i = lane & 15, kk = lane >> 4;
    u16* prm = (u16*)(sm + 8192);
    if (t < 96) {
      const int a = t >> 4, cch = t & 15;
      const u16* src = a == 0 ? A_re : a == 1 ? A_im : a == 2 ? B_re : a == 3 ? B_im : a == 4 ? C_re : C_im;
      *(u32x4*)(prm + a * 128 + cch * 8) = *(const u32x4*)(src + (size_t)d * N_ + cch * 8);
    }
    const float dt = rbf(__expf(bf2f(log_dt[d])));
    __syncthreads();
    u16 rA[4], rAi[4], rB[4], rBi[4], rC[4], rCi[4];
#pragma unroll
    for (int s = 0; s < 4; ++s) {
      const int n = 16 * w + 4 * s + kk;
      rA[s] = prm[n]; rAi[s] = prm[128 + n]; rB[s] = prm[256 + n]; rBi[s] = prm[384 + n]; rC[s] = prm[512 + n]; rCi[s] = prm[640 + n];
    }
    STAMP(0, 3);
    f32x4 acc0 = {0, 0, 0, 0}, acc1 = {0, 0, 0, 0}, acc2 = {0, 0, 0, 0}, acc3 = {0, 0, 0, 0};
    const float fi = (float)i;
#pragma unroll
    for (int s = 0; s < 4; ++s) {
      const float ar = dt * bf2f(rA[s]), ai = dt * bf2f(rAi[s]);
      const float dbr = dt * bf2f(rB[s]), dbi = dt * bf2f(rBi[s]);
      const float cr0 = bf2f(rC[s]), ci0 = bf2f(rCi[s]);
      float cr, ci;
      cmul(cr0, ci0, dbr, dbi, cr, ci);
      float er, ei, xr, xi, gr, gi, pr, pi, qr, qi;
      cexp_l(ar, ai, fi, er, ei);
      cexp_l(ar, ai, 16.f * fi, xr, xi);
      cexp_l(ar, ai, 256.f, gr, gi);
      cmul(xr, xi, cr, ci, pr, pi);
      acc0 = __builtin_amdgcn_mfma_f32_16x16x4f32(pr, er, acc0, 0, 0, 0);
      acc0 = __builtin_amdgcn_mfma_f32_16x16x4f32(-pi, ei, acc0, 0, 0, 0);
      csq(er, ei); csq(xr, xi);
      cmul(gr, gi, cr, ci, qr, qi);
      cmul(xr, xi, qr, qi, pr, pi);
      acc1 = __builtin_amdgcn_mfma_f32_16x16x4f32(pr, er, acc1, 0, 0, 0);
      acc1 = __builtin_amdgcn_mfma_f32_16x16x4f32(-pi, ei, acc1, 0, 0, 0);
      csq(er, ei); csq(xr, xi); csq(gr, gi);
      cmul(gr, gi, cr, ci, qr, qi);
      cmul(xr, xi, qr, qi, pr, pi);
      acc2 = __builtin_amdgcn_mfma_f32_16x16x4f32(pr, er, acc2, 0, 0, 0);
      acc2 = __builtin_amdgcn_mfma_f32_16x16x4f32(-pi, ei, acc2, 0, 0, 0);
      csq(er, ei); csq(xr, xi); csq(gr, gi);
      cmul(gr, gi, cr, ci, qr, qi);
      cmul(xr, xi, qr, qi, pr, pi);
      acc3 = __builtin_amdgcn_mfma_f32_16x16x4f32(pr, er, acc3, 0, 0, 0);
      acc3 = __builtin_amdgcn_mfma_f32_16x16x4f32(-pi, ei, acc3, 0, 0, 0);
    }
    STAMP(0, 1);
    float* red = sm;
#pragma unroll
    for (int j = 0; j < 4; ++j) {
      const int idx = 16 * (4 * kk + j) + i;
      red[(w * 4 + 0) * 256 + idx] = acc0[j];
      red[(w * 4 + 1) * 256 + idx] = acc1[j];
      red[(w * 4 + 2) * 256 + idx] = acc2[j];
      red[(w * 4 + 3) * 256 + idx] = acc3[j];
    }
    __syncthreads();
    STAMP(0, 4);
    float* kd = (float*)(ws + OFF_KD) + (size_t)d * KD_STRIDE;
    for (int ci = t; ci < 641; ci += 512) {
      int tile, idx;
      if (ci < 256) { tile = 0; idx = ci; }
      else if (ci < 384) { tile = 1; idx = ci - 256; }
      else if (ci < 512) { tile = 2; idx = ci - 384; }
      else { tile = 3; idx = ci - 512; }
      float v = 0.f;
#pragma unroll
      for (int ww = 0; ww < 8; ++ww) v += red[(ww * 4 + tile) * 256 + idx];
      kd[ci] = v;
    }
    STAMP(0, 2);
    return;
  }
  bid -= K1_KGEN;
  if (bid < K1_LN) {
    const int l0 = ((bid & 7) * 32 + (bid >> 3)) * 8;
    const int w = t >> 6, lane = t & 63;
    const unsigned* xr = (const unsigned*)(x + (size_t)(l0 + w) * D_);
    unsigned v[5];
#pragma unroll
    for (int j = 0; j < 5; ++j) v[j] = xr[lane + 64 * j];
    float s = 0.f;
#pragma unroll
    for (int j = 0; j < 5; ++j) s += bflo(v[j]) + bfhi(v[j]);
    STAMP(0, 3);
#pragma unroll
    for (int o = 1; o < 64; o <<= 1) s += __shfl_xor(s, o);
    const float mean = s * (1.f / 640.f);
    float q = 0.f;
#pragma unroll
    for (int j = 0; j < 5; ++j) {
      float a = bflo(v[j]) - mean, b = bfhi(v[j]) - mean;
      q += a * a + b * b;
    }
#pragma unroll
    for (int o = 1; o < 64; o <<= 1) q += __shfl_xor(q, o);
    const float mb = rbf(mean);
    const float den = rbf(sqrtf(rbf(rbf(q * (1.f / 640.f)) + 1e-5f)));
    const float rden = frcp(den);
    unsigned* sn = (unsigned*)sm;
#pragma unroll
    for (int j = 0; j < 5; ++j) {
      float a = fdiv(rbf(bflo(v[j]) - mb), den, rden), b = fdiv(rbf(bfhi(v[j]) - mb), den, rden);
      sn[w * 320 + lane + 64 * j] = pack2(a, b);
    }
    __syncthreads();
    STAMP(0, 4);
    const u16* sn16 = (const u16*)sn;
    u16* xnT = (u16*)(ws + OFF_XNT);
    for (int dd = t; dd < 640; dd += 512) {
      u32x4 a;
#pragma unroll
      for (int e = 0; e < 4; ++e)
        a[e] = (unsigned)sn16[(2 * e) * 640 + dd] | ((unsigned)sn16[(2 * e + 1) * 640 + dd] << 16);
      *(u32x4*)(xnT + (size_t)dd * L_ + l0) = a;
    }
    STAMP(0, 2);
    return;
  }
  bid -= K1_LN;
  if (bid < K1_MOD) {
    const int j0 = bid * 32;
    const int cc = t & 3, rgp = t >> 2;
    u32x4 wv[5];
    u16 cv[5];
#pragma unroll
    for (int r = 0; r < 5; ++r) {
      const int i = rgp * 5 + r;
      wv[r] = *(const u32x4*)(w_mod + (size_t)i * 3840 + j0 + 8 * cc);
      cv[r] = cvec[i];
    }
    STAMP(0, 3);
    float acc[8];
#pragma unroll
    for (int e = 0; e < 8; ++e) acc[e] = 0.f;
#pragma unroll
    for (int r = 0; r < 5; ++r) {
      const float c_ = bf2f(cv[r]);
      const float sv = rbf(c_ / (1.f + __expf(-c_)));
#pragma unroll
      for (int e = 0; e < 4; ++e) {
        acc[2 * e] += sv * bflo(wv[r][e]);
        acc[2 * e + 1] += sv * bfhi(wv[r][e]);
      }
    }
    float* red = sm;
#pragma unroll
    for (int e = 0; e < 8; ++e) red[rgp * 33 + 8 * cc + e] = acc[e];
    __syncthreads();
    {
      const int col = t >> 4, part = t & 15;
      float s = 0.f;
#pragma unroll
      for (int r = 0; r < 8; ++r) s += red[(part * 8 + r) * 33 + col];
#pragma unroll
      for (int o = 1; o < 16; o <<= 1) s += __shfl_xor(s, o);
      if (part == 0) ((float*)(ws + OFF_MOD))[j0 + col] = rbf(s);
    }
    STAMP(0, 2);
    return;
  }
  bid -= K1_MOD;
  {
    const int task = bid * 512 + t;
    if (task >= 3 * 17280) return;
    const int which = task / 17280, tk = task % 17280;
    u32x4 v[8], o[8];
    if (which < 2) {
      const int nc = tk % 216, kc = tk / 216;
      const u16* src = which == 0 ? w1 : w3;
#pragma unroll
      for (int i = 0; i < 8; ++i) v[i] = *(const u32x4*)(src + (size_t)(8 * kc + i) * F_ + 8 * nc);
      transpose8x8(v, o);
      const int nt = nc >> 1, kt = kc >> 2, g = kc & 3;
      char* dst = ws + OFF_W13F + ((size_t)((nt * 20 + kt) * 2 + which)) * 1024 + (size_t)(16 * g + 8 * (nc & 1)) * 16;
#pragma unroll
      for (int c = 0; c < 8; ++c) *(u32x4*)(dst + c * 16) = o[c];
    } else {
      const int nc = tk % 80, kc = tk / 80;
#pragma unroll
      for (int i = 0; i < 8; ++i) v[i] = *(const u32x4*)(w2 + (size_t)(8 * kc + i) * D_ + 8 * nc);
      transpose8x8(v, o);
      const int nt = nc >> 1, kt = kc >> 2, g = kc & 3;
      char* dst = ws + OFF_W2F + ((size_t)(nt * 54 + kt)) * 1024 + (size_t)(16 * g + 8 * (nc & 1)) * 16;
#pragma unroll
      for (int c = 0; c < 8; ++c) *(u32x4*)(dst + c * 16) = o[c];
    }
    STAMP(0, 2);
  }
}

constexpr int UC_STRIDE = 4608;
constexpr int UC_BYTES = 8 * UC_STRIDE;
constexpr int KR_BYTES = 384 * 16;
constexpr int K2_LDS = UC_BYTES + 2 * KR_BYTES;

DI int lag_to_ci(int lag) {
  const int r = (int)rbf((float)lag);
  if (r < 256) return r;
  if (r < 512) return 256 + ((r - 256) >> 1);
  if (r < 1024) return 384 + ((r - 512) >> 2);
  return 512 + ((r - 1024) >> 3);
}

__global__ __launch_bounds__(512) void k2_conv(const u16* __restrict__ xnT, const float* __restrict__ KD,
                                               const float* __restrict__ modv, const u16* __restrict__ Dskip,
                                               u16* __restrict__ gyT) {
  __shared__ __attribute__((aligned(16))) char sm[K2_LDS];
  char* uc = sm;
  char* krh = sm + UC_BYTES;
  char* krl = krh + KR_BYTES;
  const int d = blockIdx.x, t = threadIdx.x;
  const int w = t >> 6, lane = t & 63, c = lane & 15, g = lane >> 4;
  STAMP(1, 0);
  const float* kd = KD + (size_t)d * KD_STRIDE;
  const float sa = modv[d], ca1 = rbf(1.f + modv[640 + d]), ga = modv[1280 + d];
  const float dsk = bf2f(Dskip[d]), k0 = kd[0];
  const u32x2 xn_own = ((const u32x2*)(xnT + (size_t)d * L_))[t];

  if (t < 256) {
    const u32x4* xn4 = (const u32x4*)(xnT + (size_t)d * L_);
    u32x4 cur = xn4[t];
    u32x4 prv = {0, 0, 0, 0};
    if (t > 0) prv = xn4[t - 1];
    unsigned hv[8];
#pragma unroll
    for (int e = 0; e < 4; ++e) {
      float a = rbf(rbf(bflo(prv[e]) * ca1) + sa), b = rbf(rbf(bfhi(prv[e]) * ca1) + sa);
      hv[e] = (t > 0) ? pack2(a, b) : 0u;
      float a2 = rbf(rbf(bflo(cur[e]) * ca1) + sa), b2 = rbf(rbf(bfhi(cur[e]) * ca1) + sa);
      hv[4 + e] = pack2(a2, b2);
    }
#pragma unroll
    for (int s = 0; s < 8; ++s) {
      u32x4 o;
      if ((s & 1) == 0) {
#pragma unroll
        for (int e = 0; e < 4; ++e) o[e] = hv[s / 2 + e];
      } else {
#pragma unroll
        for (int e = 0; e < 4; ++e) o[e] = (hv[s / 2 + e] >> 16) | (hv[s / 2 + e + 1] << 16);
      }
      *(u32x4*)(uc + s * UC_STRIDE + 64 * (s & 3) + 32 * (s >> 2) + 16 * (t + 7)) = o;
    }
  } else {
    const int t2 = t - 256;
    float kv[8];
#pragma unroll
    for (int u = 0; u < 8; ++u) {
      const int lag = 2048 - (8 * t2 + u);
      kv[u] = kd[lag_to_ci(lag < 2048 ? lag : 2047)];
      if (lag == 2048) kv[u] = 0.f;
    }
    if (t2 < 56) {
      const int s = t2 / 7, m = t2 % 7;
      *(u32x4*)(uc + s * UC_STRIDE + 64 * (s & 3) + 32 * (s >> 2) + 16 * m) = u32x4{0, 0, 0, 0};
    }
    if (t2 < 120) {
      *(u32x4*)(krh + 16 * (264 + t2)) = u32x4{0, 0, 0, 0};
      *(u32x4*)(krl + 16 * (264 + t2)) = u32x4{0, 0, 0, 0};
    }
    u32x4 kh, kl;
#pragma unroll
    for (int e = 0; e < 4; ++e) {
      const float h0 = rbf(kv[2 * e]), h1 = rbf(kv[2 * e + 1]);
      kh[e] = pack2(h0, h1);
      kl[e] = pack2(kv[2 * e] - h0, kv[2 * e + 1] - h1);
    }
    const int ci = 8 + t2, R = ci >> 3, e = ci & 7;
    const int pos = 8 * R + (e ^ (R & 7));
    *(u32x4*)(krh + 16 * pos) = kh;
    *(u32x4*)(krl + 16 * pos) = kl;
  }
  __syncthreads();

  STAMP(1, 1);
  f32x4 acc[2][4];
#pragma unroll
  for (int a = 0; a < 2; ++a)
#pragma unroll
    for (int b = 0; b < 4; ++b) acc[a][b] = f32x4{0, 0, 0, 0};

  const int sg = c & 7;
  const char* ub = uc + sg * UC_STRIDE + 64 * (sg & 3) + 32 * (sg >> 2) + 16 * ((c >> 3) + g);
  int dA_lo, dA_hi, s_lo, s_hi;
  if (w < 5) { dA_lo = 6 * w; dA_hi = 6 * w + 6; s_lo = 32; s_hi = 32; }
  else if (w == 5) { dA_lo = 30; dA_hi = 32; s_lo = 32; s_hi = 40; }
  else { dA_lo = 32; dA_hi = 32; s_lo = 40 + 12 * (w - 6); s_hi = s_lo + 12; }
  for (int kp = dA_lo; kp < dA_hi; ++kp) {
    bf16x8 bfr[4];
#pragma unroll
    for (int ct = 0; ct < 4; ++ct) bfr[ct] = *(const bf16x8*)(ub + 16 * (2 * ct + 4 * kp));
    const int e = 4 * (kp & 1) + g;
    bf16x8 ah[2], al[2];
#pragma unroll
    for (int tt = 0; tt < 2; ++tt) {
      const int R = 32 - 16 * tt - c + (kp >> 1);
      const int pos = 8 * R + (e ^ (R & 7));
      ah[tt] = *(const bf16x8*)(krh + 16 * pos);
      al[tt] = *(const bf16x8*)(krl + 16 * pos);
    }
#pragma unroll
    for (int tt = 0; tt < 2; ++tt)
#pragma unroll
      for (int ct = 0; ct < 4; ++ct) {
        acc[tt][ct] = __builtin_amdgcn_mfma_f32_16x16x32_bf16(ah[tt], bfr[ct], acc[tt][ct], 0, 0, 0);
        acc[tt][ct] = __builtin_amdgcn_mfma_f32_16x16x32_bf16(al[tt], bfr[ct], acc[tt][ct], 0, 0, 0);
      }
  }
  for (int kp = s_lo; kp < s_hi; ++kp) {
    bf16x8 bfr[4];
#pragma unroll
    for (int ct = 0; ct < 4; ++ct) bfr[ct] = *(const bf16x8*)(ub + 16 * (2 * ct + 4 * kp));
    const int e = 4 * (kp & 1) + g;
    const int R = 16 - c + (kp >> 1);
    const int pos = 8 * R + (e ^ (R & 7));
    const bf16x8 ah = *(const bf16x8*)(krh + 16 * pos);
    const bf16x8 al = *(const bf16x8*)(krl + 16 * pos);
#pragma unroll
    for (int ct = 0; ct < 4; ++ct) {
      acc[1][ct] = __builtin_amdgcn_mfma_f32_16x16x32_bf16(ah, bfr[ct], acc[1][ct], 0, 0, 0);
      acc[1][ct] = __builtin_amdgcn_mfma_f32_16x16x32_bf16(al, bfr[ct], acc[1][ct], 0, 0, 0);
    }
  }
  __syncthreads();
  STAMP(1, 2);
  float* yb_ = (float*)sm;
  if (w >= 4) {
#pragma unroll
    for (int tt = 0; tt < 2; ++tt)
#pragma unroll
      for (int ct = 0; ct < 4; ++ct) *(f32x4*)(yb_ + (((w - 4) * 8 + tt * 4 + ct) * 64 + lane) * 4) = acc[tt][ct];
  }
  __syncthreads();
  if (w < 4) {
#pragma unroll
    for (int tt = 0; tt < 2; ++tt)
#pragma unroll
      for (int ct = 0; ct < 4; ++ct) acc[tt][ct] += *(const f32x4*)(yb_ + ((w * 8 + tt * 4 + ct) * 64 + lane) * 4);
  }
  __syncthreads();
  if (w < 4) {
    float* yp = yb_ + w * 2048;
#pragma unroll
    for (int tt = 0; tt < 2; ++tt)
#pragma unroll
      for (int ct = 0; ct < 4; ++ct)
#pragma unroll
        for (int j = 0; j < 4; ++j) yp[64 * (16 * tt + 4 * g + j) + 16 * ct + c] = acc[tt][ct][j];
  }
  __syncthreads();
  const float* y0 = yb_ + 4 * t;
  f32x4 s0 = *(const f32x4*)(y0);
#pragma unroll
  for (int ww = 1; ww < 4; ++ww) s0 += *(const f32x4*)(y0 + ww * 2048);
  float r[4];
#pragma unroll
  for (int i = 0; i < 4; ++i) {
    const float xn = (i & 1) ? bfhi(xn_own[i >> 1]) : bflo(xn_own[i >> 1]);
    const float hh = rbf(rbf(xn * ca1) + sa);
    const float yb = rbf(s0[i] + k0 * hh);
    const float sk = rbf(hh * dsk);
    const float sy = rbf(yb + sk);
    r[i] = ga * sy;
  }
  ((u32x2*)(gyT + (size_t)d * L_))[t] = u32x2{pack2(r[0], r[1]), pack2(r[2], r[3])};
  STAMP(1, 3);
}

__global__ __launch_bounds__(320) void k3_ln2(const u16* __restrict__ gyT, const u16* __restrict__ x,
                                              const float* __restrict__ modv, u16* __restrict__ x1,
                                              char* __restrict__ h2F) {
  __shared__ float red[16 * 81];
  __shared__ float stat[32];
  const int t = threadIdx.x;
  const int l0 = ((blockIdx.x & 7) * 16 + (blockIdx.x >> 3)) * 16;
  STAMP(2, 0);
  const int cg = t & 3, ch = t >> 2;
  u32x2 v[8];
#pragma unroll
  for (int i = 0; i < 8; ++i) v[i] = *(const u32x2*)(gyT + (size_t)(8 * ch + i) * L_ + l0 + 4 * cg);
  u32x4 xr[4];
#pragma unroll
  for (int c = 0; c < 4; ++c) xr[c] = *(const u32x4*)(x + (size_t)(l0 + 4 * cg + c) * D_ + 8 * ch);
  const f32x4 sb0 = *(const f32x4*)(modv + 3 * 640 + 8 * ch), sb1 = *(const f32x4*)(modv + 3 * 640 + 8 * ch + 4);
  const f32x4 cb0 = *(const f32x4*)(modv + 4 * 640 + 8 * ch), cb1 = *(const f32x4*)(modv + 4 * 640 + 8 * ch + 4);
  u32x4 o[4];
#pragma unroll
  for (int dd = 0; dd < 2; ++dd)
#pragma unroll
    for (int p = 0; p < 4; ++p) {
      unsigned a = v[2 * p][dd], b = v[2 * p + 1][dd];
      o[2 * dd][p] = pack2(bflo(xr[2 * dd][p]) + bflo(a), bfhi(xr[2 * dd][p]) + bflo(b));
      o[2 * dd + 1][p] = pack2(bflo(xr[2 * dd + 1][p]) + bfhi(a), bfhi(xr[2 * dd + 1][p]) + bfhi(b));
    }
#pragma unroll
  for (int c = 0; c < 4; ++c) {
    float s = 0.f;
#pragma unroll
    for (int e = 0; e < 4; ++e) s += bflo(o[c][e]) + bfhi(o[c][e]);
    red[(4 * cg + c) * 81 + ch] = s;
  }
  __syncthreads();
  if (t < 256) {
    const int row = t >> 4, part = t & 15;
    float s = 0.f;
#pragma unroll
    for (int e = 0; e < 5; ++e) s += red[row * 81 + part * 5 + e];
#pragma unroll
    for (int of = 1; of < 16; of <<= 1) s += __shfl_xor(s, of);
    if (part == 0) stat[row] = s * (1.f / 640.f);
  }
  __syncthreads();
  STAMP(2, 1);
  float mean[4];
#pragma unroll
  for (int c = 0; c < 4; ++c) {
    mean[c] = stat[4 * cg + c];
    float s = 0.f;
#pragma unroll
    for (int e = 0; e < 4; ++e) {
      float a = bflo(o[c][e]) - mean[c], b = bfhi(o[c][e]) - mean[c];
      s += a * a + b * b;
    }
    red[(4 * cg + c) * 81 + ch] = s;
  }
  __syncthreads();
  if (t < 256) {
    const int row = t >> 4, part = t & 15;
    float s = 0.f;
#pragma unroll
    for (int e = 0; e < 5; ++e) s += red[row * 81 + part * 5 + e];
#pragma unroll
    for (int of = 1; of < 16; of <<= 1) s += __shfl_xor(s, of);
    if (part == 0) stat[16 + row] = s * (1.f / 640.f);
  }
  __syncthreads();
  STAMP(2, 2);
  float sbv[8] = {sb0[0], sb0[1], sb0[2], sb0[3], sb1[0], sb1[1], sb1[2], sb1[3]};
  float cbv[8] = {cb0[0], cb0[1], cb0[2], cb0[3], cb1[0], cb1[1], cb1[2], cb1[3]};
#pragma unroll
  for (int i = 0; i < 8; ++i) cbv[i] = rbf(1.f + cbv[i]);
  const int kt = ch >> 2, gq = ch & 3;
#pragma unroll
  for (int c = 0; c < 4; ++c) {
    const int m = l0 + 4 * cg + c;
    const float mb = rbf(mean[c]);
    const float den = rbf(sqrtf(rbf(rbf(stat[16 + 4 * cg + c]) + 1e-5f)));
    const float rden = frcp(den);
    u32x4 hv;
#pragma unroll
    for (int e = 0; e < 4; ++e) {
      float a = rbf(fdiv(rbf(bflo(o[c][e]) - mb), den, rden)), b = rbf(fdiv(rbf(bfhi(o[c][e]) - mb), den, rden));
      a = rbf(a * cbv[2 * e]) + sbv[2 * e];
      b = rbf(b * cbv[2 * e + 1]) + sbv[2 * e + 1];
      hv[e] = pack2(a, b);
    }
    *(u32x4*)(h2F + ((size_t)((m >> 4) * 20 + kt)) * 1024 + (size_t)((m & 15) + 16 * gq) * 16) = hv;
    *(u32x4*)(x1 + (size_t)m * D_ + 8 * ch) = o[c];
  }
  STAMP(2, 3);
}

constexpr int G1_SLOT = 24576, G1_PF = 4;

__global__ __launch_bounds__(512) void k4_gemm1(const char* __restrict__ h2F, const char* __restrict__ W13F,
                                                char* __restrict__ hidF) {
  __shared__ __attribute__((aligned(16))) char sm[2 * G1_SLOT];
  const int t = threadIdx.x, w = t >> 6, lane = t & 63;
  const int xcd = blockIdx.x & 7, idx = blockIdx.x >> 3;
  const int nq = xcd >> 1, nstart = 7 * nq, ncnt = (nq == 3) ? 6 : 7;
  if (idx >= 4 * ncnt) return;
  const int bm = 4 * (xcd & 1) + (idx & 3), bn = nstart + (idx >> 2);
  const int wm = w >> 1, wn = w & 1;
  STAMP(3, 0);
  const char* hsrc = h2F + ((size_t)((16 * bm + 2 * w) * 20)) * 1024 + lane * 16;
  const char* wsrc = W13F + ((size_t)((4 * bn + (w >> 1)) * 20)) * 2048 + (w & 1) * 1024 + lane * 16;
  f32x4 acc[2][2][4];
#pragma unroll
  for (int a = 0; a < 2; ++a)
#pragma unroll
    for (int b = 0; b < 2; ++b)
#pragma unroll
      for (int cI = 0; cI < 4; ++cI) acc[a][b][cI] = f32x4{0, 0, 0, 0};
  u32x4 pf[G1_PF][3];
#pragma unroll
  for (int s = 0; s < G1_PF; ++s) {
    pf[s][0] = *(const u32x4*)(hsrc + (size_t)s * 1024);
    pf[s][1] = *(const u32x4*)(hsrc + (size_t)s * 1024 + 20 * 1024);
    pf[s][2] = *(const u32x4*)(wsrc + (size_t)s * 2048);
  }
  {
    char* base = sm;
    *(u32x4*)(base + (2 * w) * 1024 + lane * 16) = pf[0][0];
    *(u32x4*)(base + (2 * w + 1) * 1024 + lane * 16) = pf[0][1];
    *(u32x4*)(base + 16384 + w * 1024 + lane * 16) = pf[0][2];
    pf[0][0] = *(const u32x4*)(hsrc + (size_t)G1_PF * 1024);
    pf[0][1] = *(const u32x4*)(hsrc + (size_t)G1_PF * 1024 + 20 * 1024);
    pf[0][2] = *(const u32x4*)(wsrc + (size_t)G1_PF * 2048);
  }
#pragma unroll
  for (int s = 0; s < 20; ++s) {
    __syncthreads();
    if (s + 1 < 20) {
      char* nb = sm + ((s + 1) & 1) * G1_SLOT;
      const int r = (s + 1) % G1_PF;
      *(u32x4*)(nb + (2 * w) * 1024 + lane * 16) = pf[r][0];
      *(u32x4*)(nb + (2 * w + 1) * 1024 + lane * 16) = pf[r][1];
      *(u32x4*)(nb + 16384 + w * 1024 + lane * 16) = pf[r][2];
      if (s + 1 + G1_PF < 20) {
        pf[r][0] = *(const u32x4*)(hsrc + (size_t)(s + 1 + G1_PF) * 1024);
        pf[r][1] = *(const u32x4*)(hsrc + (size_t)(s + 1 + G1_PF) * 1024 + 20 * 1024);
        pf[r][2] = *(const u32x4*)(wsrc + (size_t)(s + 1 + G1_PF) * 2048);
      }
    }
    const char* base = sm + (s & 1) * G1_SLOT;
    bf16x8 hb[4], wa[2][2];
#pragma unroll
    for (int i = 0; i < 4; ++i) hb[i] = *(const bf16x8*)(base + (4 * wm + i) * 1024 + lane * 16);
#pragma unroll
    for (int a = 0; a < 2; ++a)
#pragma unroll
      for (int b = 0; b < 2; ++b)
        wa[a][b] = *(const bf16x8*)(base + 16384 + ((2 * wn + a) * 2 + b) * 1024 + lane * 16);
#pragma unroll
    for (int a = 0; a < 2; ++a)
#pragma unroll
      for (int b = 0; b < 2; ++b)
#pragma unroll
        for (int i = 0; i < 4; ++i)
          acc[a][b][i] = __builtin_amdgcn_mfma_f32_16x16x32_bf16(wa[a][b], hb[i], acc[a][b][i], 0, 0, 0);
  }
  STAMP(3, 1);
  const int cl = lane & 15, g = lane >> 4;
#pragma unroll
  for (int a = 0; a < 2; ++a) {
    const int nt = 4 * bn + 2 * wn + a;
    const int kt2 = nt >> 1;
    const int slot = 16 * (2 * (nt & 1) + (g >> 1)) + cl;
#pragma unroll
    for (int i = 0; i < 4; ++i) {
      const int mt = 16 * bm + 4 * wm + i;
      float hv[4];
#pragma unroll
      for (int j = 0; j < 4; ++j) {
        const float gv = rbf(acc[a][0][i][j]), uv = rbf(acc[a][1][i][j]);
        const float dn = 1.f + __expf(-gv);
        const float sv = rbf(fdiv(gv, dn, frcp(dn)));
        hv[j] = sv * uv;
      }
      u32x2 pv = {pack2(hv[0], hv[1]), pack2(hv[2], hv[3])};
      *(u32x2*)(hidF + ((size_t)(mt * 54 + kt2)) * 1024 + slot * 16 + (g & 1) * 8) = pv;
    }
  }
  STAMP(3, 2);
}

__global__ __launch_bounds__(512) void k5_gemm2(const char* __restrict__ hidF, const char* __restrict__ W2F,
                                                const u16* __restrict__ x1, const float* __restrict__ modv,
                                                u16* __restrict__ out) {
  __shared__ __attribute__((aligned(16))) float sm[8 * 20 * 256];
  const int t = threadIdx.x, w = t >> 6, lane = t & 63;
  const int bn = (blockIdx.x >> 3) & 7, bm = 4 * (blockIdx.x & 7) + (blockIdx.x >> 6);
  const int k_lo = (w < 6) ? 7 * w : 42 + 6 * (w - 6);
  const int nk = (w < 6) ? 7 : 6;
  f32x4 acc[5][4];
#pragma unroll
  for (int a = 0; a < 5; ++a)
#pragma unroll
    for (int b = 0; b < 4; ++b) acc[a][b] = f32x4{0, 0, 0, 0};
  STAMP(4, 0);
  const char* hp = hidF + ((size_t)(4 * bm) * 54 + k_lo) * 1024 + lane * 16;
  const char* wp = W2F + ((size_t)(5 * bn) * 54 + k_lo) * 1024 + lane * 16;
  bf16x8 hb[3][4], wa[3][5];
#pragma unroll
  for (int p = 0; p < 2; ++p) {
#pragma unroll
    for (int i = 0; i < 4; ++i) hb[p][i] = *(const bf16x8*)(hp + (size_t)(i * 54 + p) * 1024);
#pragma unroll
    for (int i = 0; i < 5; ++i) wa[p][i] = *(const bf16x8*)(wp + (size_t)(i * 54 + p) * 1024);
  }
  const int c = lane & 15, g = lane >> 4;
  const int t_lo = (w < 4) ? 3 * w : 12 + 2 * (w - 4);
  const int t_n = (w < 4) ? 3 : 2;
  u32x2 xv[3];
  f32x4 gbv[3];
#pragma unroll
  for (int q = 0; q < 3; ++q) {
    const int tile = (q < t_n) ? t_lo + q : t_lo;
    const int a = tile >> 2, b = tile & 3;
    const int m = 16 * (4 * bm + b) + c;
    const int n = 16 * (5 * bn + a) + 4 * g;
    xv[q] = *(const u32x2*)(x1 + (size_t)m * D_ + n);
    gbv[q] = *(const f32x4*)(modv + 5 * 640 + n);
  }
#pragma unroll
  for (int k = 0; k < 7; ++k) {
    if (k < nk) {
      if (k + 2 < nk) {
#pragma unroll
        for (int i = 0; i < 4; ++i) hb[(k + 2) % 3][i] = *(const bf16x8*)(hp + (size_t)(i * 54 + k + 2) * 1024);
#pragma unroll
        for (int i = 0; i < 5; ++i) wa[(k + 2) % 3][i] = *(const bf16x8*)(wp + (size_t)(i * 54 + k + 2) * 1024);
      }
#pragma unroll
      for (int a = 0; a < 5; ++a)
#pragma unroll
        for (int b = 0; b < 4; ++b)
          acc[a][b] = __builtin_amdgcn_mfma_f32_16x16x32_bf16(wa[k % 3][a], hb[k % 3][b], acc[a][b], 0, 0, 0);
    }
  }
  STAMP(4, 1);
#pragma unroll
  for (int a = 0; a < 5; ++a)
#pragma unroll
    for (int b = 0; b < 4; ++b) *(f32x4*)(sm + ((w * 20 + a * 4 + b) * 64 + lane) * 4) = acc[a][b];
  __syncthreads();
  STAMP(4, 2);
#pragma unroll
  for (int q = 0; q < 3; ++q) {
    if (q < t_n) {
      const int tile = t_lo + q;
      const int a = tile >> 2, b = tile & 3;
      const int m = 16 * (4 * bm + b) + c;
      const int n = 16 * (5 * bn + a) + 4 * g;
      f32x4 s = *(const f32x4*)(sm + ((0 * 20 + tile) * 64 + lane) * 4);
#pragma unroll
      for (int ww = 1; ww < 8; ++ww) s += *(const f32x4*)(sm + ((ww * 20 + tile) * 64 + lane) * 4);
      float r0 = bflo(xv[q][0]) + rbf(gbv[q][0] * rbf(s[0]));
      float r1 = bfhi(xv[q][0]) + rbf(gbv[q][1] * rbf(s[1]));
      float r2 = bflo(xv[q][1]) + rbf(gbv[q][2] * rbf(s[2]));
      float r3 = bfhi(xv[q][1]) + rbf(gbv[q][3] * rbf(s[3]));
      u32x2 ov = {pack2(r0, r1), pack2(r2, r3)};
      *(u32x2*)(out + (size_t)m * D_ + n) = ov;
    }
  }
  STAMP(4, 3);
}

extern "C" void kernel_launch(void* const* d_in, const int* in_sizes, int n_in, void* d_out, int out_size,
                              void* d_ws, size_t ws_size, hipStream_t stream) {
  const u16* x = (const u16*)d_in[0];
  const u16* A_re = (const u16*)d_in[1];
  const u16* A_im = (const u16*)d_in[2];
  const u16* B_re = (const u16*)d_in[3];
  const u16* B_im = (const u16*)d_in[4];
  const u16* C_re = (const u16*)d_in[5];
  const u16* C_im = (const u16*)d_in[6];
  const u16* log_dt = (const u16*)d_in[7];
  const u16* Dskip = (const u16*)d_in[8];
  const u16* w1 = (const u16*)d_in[10];
  const u16* w3 = (const u16*)d_in[11];
  const u16* w2 = (const u16*)d_in[12];
  const u16* cvec = (const u16*)d_in[13];
  const u16* w_mod = (const u16*)d_in[14];
  char* ws = (char*)d_ws;
  u16* out = (u16*)d_out;

  k1_prep<<<K1_KGEN + K1_LN + K1_MOD + K1_RT, 512, 0, stream>>>(x, A_re, A_im, B_re, B_im, C_re, C_im, log_dt,
                                                                 w1, w3, w2, cvec, w_mod, ws);
  k2_conv<<<640, 512, 0, stream>>>((const u16*)(ws + OFF_XNT), (const float*)(ws + OFF_KD),
                                   (const float*)(ws + OFF_MOD), Dskip, (u16*)(ws + OFF_GYT));
  k3_ln2<<<128, 320, 0, stream>>>((const u16*)(ws + OFF_GYT), x, (const float*)(ws + OFF_MOD), (u16*)(ws + OFF_X1),
                                  ws + OFF_H2F);
  k4_gemm1<<<8 * 28, 512, 0, stream>>>(ws + OFF_H2F, ws + OFF_W13F, ws + OFF_HIDF);
  k5_gemm2<<<32 * 8, 512, 0, stream>>>(ws + OFF_HIDF, ws + OFF_W2F, (const u16*)(ws + OFF_X1),
                                       (const float*)(ws + OFF_MOD), out);
}
```

```cpp
#include <hip/hip_runtime.h>
#include <hip/hip_bf16.h>

typedef __attribute__((ext_vector_type(8))) short bf16x8;
typedef __attribute__((ext_vector_type(4))) float f32x4;
typedef __attribute__((ext_vector_type(4))) unsigned u32x4;
typedef __attribute__((ext_vector_type(2))) unsigned u32x2;
typedef unsigned short u16;

#define DI __device__ __forceinline__
#define STAMP(K, I)

constexpr int L_ = 2048, D_ = 640, N_ = 128, F_ = 1728;

constexpr size_t OFF_MOD = 0;
constexpr size_t OFF_K0 = 16384;
constexpr size_t OFF_XNT = 32768;
constexpr size_t SZ_ACT = (size_t)D_ * L_ * 2;
constexpr int KD_STRIDE = 648;
constexpr size_t OFF_KD = OFF_XNT + SZ_ACT;
constexpr size_t OFF_GYT = OFF_KD + (size_t)D_ * KD_STRIDE * 4;
constexpr size_t OFF_X1 = OFF_GYT + SZ_ACT;
constexpr size_t OFF_H2F = OFF_X1 + SZ_ACT;
constexpr size_t OFF_W13F = OFF_H2F + SZ_ACT;
constexpr size_t SZ_W = (size_t)D_ * F_ * 2;
constexpr size_t OFF_W2F = OFF_W13F + 2 * SZ_W;
constexpr size_t OFF_HIDF = OFF_W2F + SZ_W;

DI float bf2f(unsigned v) { return __uint_as_float(v << 16); }
DI float bflo(unsigned v) { return __uint_as_float(v << 16); }
DI float bfhi(unsigned v) { return __uint_as_float(v & 0xffff0000u); }
DI float rbf(float x) { return (float)(__bf16)x; }
DI unsigned f2bf(float x) { __bf16 b = (__bf16)x; return (unsigned)__builtin_bit_cast(u16, b); }
DI unsigned pack2(float lo, float hi) { return f2bf(lo) | (f2bf(hi) << 16); }
DI float frcp(float x) { return __builtin_amdgcn_rcpf(x); }
DI float fdiv(float a, float b, float rb) { float q = a * rb; float r = fmaf(-q, b, a); return fmaf(r, rb, q); }

DI void transpose8x8(const u32x4* in, u32x4* out) {
#pragma unroll
  for (int d = 0; d < 4; ++d) {
#pragma unroll
    for (int p = 0; p < 4; ++p) {
      unsigned a = in[2 * p][d], b = in[2 * p + 1][d];
      out[2 * d][p] = (a & 0xffffu) | (b << 16);
      out[2 * d + 1][p] = (a >> 16) | (b & 0xffff0000u);
    }
  }
}

typedef __attribute__((ext_vector_type(2))) float c32;
DI c32 cexp_l(float ar, float ai, float l) {
  const float mag = __expf(ar * l);
  float rev = (ai * l) * 0.15915494309189535f;
  rev = rev - floorf(rev);
  c32 cs = {__builtin_amdgcn_cosf(rev), __builtin_amdgcn_sinf(rev)};
  return cs * mag;
}
DI c32 cmul(c32 a, c32 b) {
  c32 bs = {-b.y, b.x};
  return a.xx * b + a.yy * bs;
}

constexpr int K1_KGEN = 640, K1_LN = 256, K1_MOD = 120, K1_RT = 102;

__global__ __launch_bounds__(512) void k1_prep(
    const u16* __restrict__ x, const u16* __restrict__ A_re, const u16* __restrict__ A_im,
    const u16* __restrict__ B_re, const u16* __restrict__ B_im, const u16* __restrict__ C_re,
    const u16* __restrict__ C_im, const u16* __restrict__ log_dt, const u16* __restrict__ w1,
    const u16* __restrict__ w3, const u16* __restrict__ w2, const u16* __restrict__ cvec,
    const u16* __restrict__ w_mod, char* __restrict__ ws) {
  __shared__ __attribute__((aligned(16))) float sm[8 * 4 * 256 + 1536];
  const int t = threadIdx.x;
  int bid = blockIdx.x;
  STAMP(0, 0);
  if (bid < K1_KGEN) {
    const int d = bid;
    const int w = t >> 6, lane = t & 63, i = lane & 15, kk = lane >> 4;
    float* prm = sm + 8192;
    if (t < 128) {
      const int n = d * N_ + t;
      const float dt = rbf(__expf(bf2f(log_dt[d])));
      const float ar = dt * bf2f(A_re[n]), ai = dt * bf2f(A_im[n]);
      const c32 db = {dt * bf2f(B_re[n]), dt * bf2f(B_im[n])};
      const c32 cc = {bf2f(C_re[n]), bf2f(C_im[n])};
      const c32 c0 = cmul(cc, db);
      c32 gg = cexp_l(ar, ai, 256.f);
      const c32 c1 = cmul(gg, c0);
      gg = cmul(gg, gg);
      const c32 c2 = cmul(gg, c0);
      gg = cmul(gg, gg);
      const c32 c3 = cmul(gg, c0);
      *(f32x4*)(prm + t * 12) = f32x4{ar, ai, c0.x, c0.y};
      *(f32x4*)(prm + t * 12 + 4) = f32x4{c1.x, c1.y, c2.x, c2.y};
      *(f32x4*)(prm + t * 12 + 8) = f32x4{c3.x, c3.y, 0.f, 0.f};
    }
    __syncthreads();
    STAMP(0, 3);
    f32x4 acc0 = {0, 0, 0, 0}, acc1 = {0, 0, 0, 0}, acc2 = {0, 0, 0, 0}, acc3 = {0, 0, 0, 0};
    const float fi = (float)i;
#pragma unroll
    for (int s = 0; s < 4; ++s) {
      const int n = 16 * w + 4 * s + kk;
      const f32x4 p0 = *(const f32x4*)(prm + n * 12), p1 = *(const f32x4*)(prm + n * 12 + 4), p2 = *(const f32x4*)(prm + n * 12 + 8);
      c32 e = cexp_l(p0[0], p0[1], fi);
      c32 xx = cexp_l(p0[0], p0[1], 16.f * fi);
      c32 pp;
      pp = cmul(xx, c32{p0[2], p0[3]});
      acc0 = __builtin_amdgcn_mfma_f32_16x16x4f32(pp.x, e.x, acc0, 0, 0, 0);
      acc0 = __builtin_amdgcn_mfma_f32_16x16x4f32(-pp.y, e.y, acc0, 0, 0, 0);
      e = cmul(e, e); xx = cmul(xx, xx);
      pp = cmul(xx, c32{p1[0], p1[1]});
      acc1 = __builtin_amdgcn_mfma_f32_16x16x4f32(pp.x, e.x, acc1, 0, 0, 0);
      acc1 = __builtin_amdgcn_mfma_f32_16x16x4f32(-pp.y, e.y, acc1, 0, 0, 0);
      e = cmul(e, e); xx = cmul(xx, xx);
      pp = cmul(xx, c32{p1[2], p1[3]});
      acc2 = __builtin_amdgcn_mfma_f32_16x16x4f32(pp.x, e.x, acc2, 0, 0, 0);
      acc2 = __builtin_amdgcn_mfma_f32_16x16x4f32(-pp.y, e.y, acc2, 0, 0, 0);
      e = cmul(e, e); xx = cmul(xx, xx);
      pp = cmul(xx, c32{p2[0], p2[1]});
      acc3 = __builtin_amdgcn_mfma_f32_16x16x4f32(pp.x, e.x, acc3, 0, 0, 0);
      acc3 = __builtin_amdgcn_mfma_f32_16x16x4f32(-pp.y, e.y, acc3, 0, 0, 0);
    }
    STAMP(0, 1);
    float* red = sm;
#pragma unroll
    for (int j = 0; j < 4; ++j) {
      const int idx = 16 * (4 * kk + j) + i;
      red[(w * 4 + 0) * 256 + idx] = acc0[j];
      red[(w * 4 + 1) * 256 + idx] = acc1[j];
      red[(w * 4 + 2) * 256 + idx] = acc2[j];
      red[(w * 4 + 3) * 256 + idx] = acc3[j];
    }
    __syncthreads();
    STAMP(0, 4);
    float* kd = (float*)(ws + OFF_KD) + (size_t)d * KD_STRIDE;
    for (int ci = t; ci < 641; ci += 512) {
      int tile, idx;
      if (ci < 256) { tile = 0; idx = ci; }
      else if (ci < 384) { tile = 1; idx = ci - 256; }
      else if (ci < 512) { tile = 2; idx = ci - 384; }
      else { tile = 3; idx = ci - 512; }
      float v = 0.f;
#pragma unroll
      for (int ww = 0; ww < 8; ++ww) v += red[(ww * 4 + tile) * 256 + idx];
      kd[ci] = v;
    }
    STAMP(0, 2);
    return;
  }
  bid -= K1_KGEN;
  if (bid < K1_LN) {
    const int l0 = ((bid & 7) * 32 + (bid >> 3)) * 8;
    const int w = t >> 6, lane = t & 63;
    const unsigned* xr = (const unsigned*)(x + (size_t)(l0 + w) * D_);
    unsigned v[5];
#pragma unroll
    for (int j = 0; j < 5; ++j) v[j] = xr[lane + 64 * j];
    float s = 0.f;
#pragma unroll
    for (int j = 0; j < 5; ++j) s += bflo(v[j]) + bfhi(v[j]);
    STAMP(0, 3);
#pragma unroll
    for (int o = 1; o < 64; o <<= 1) s += __shfl_xor(s, o);
    const float mean = s * (1.f / 640.f);
    float q = 0.f;
#pragma unroll
    for (int j = 0; j < 5; ++j) {
      float a = bflo(v[j]) - mean, b = bfhi(v[j]) - mean;
      q += a * a + b * b;
    }
#pragma unroll
    for (int o = 1; o < 64; o <<= 1) q += __shfl_xor(q, o);
    const float mb = rbf(mean);
    const float den = rbf(sqrtf(rbf(rbf(q * (1.f / 640.f)) + 1e-5f)));
    const float rden = frcp(den);
    unsigned* sn = (unsigned*)sm;
#pragma unroll
    for (int j = 0; j < 5; ++j) {
      float a = fdiv(rbf(bflo(v[j]) - mb), den, rden), b = fdiv(rbf(bfhi(v[j]) - mb), den, rden);
      sn[w * 320 + lane + 64 * j] = pack2(a, b);
    }
    __syncthreads();
    STAMP(0, 4);
    const u16* sn16 = (const u16*)sn;
    u16* xnT = (u16*)(ws + OFF_XNT);
    for (int dd = t; dd < 640; dd += 512) {
      u32x4 a;
#pragma unroll
      for (int e = 0; e < 4; ++e)
        a[e] = (unsigned)sn16[(2 * e) * 640 + dd] | ((unsigned)sn16[(2 * e + 1) * 640 + dd] << 16);
      *(u32x4*)(xnT + (size_t)dd * L_ + l0) = a;
    }
    STAMP(0, 2);
    return;
  }
  bid -= K1_LN;
  if (bid < K1_MOD) {
    const int j0 = bid * 32;
    const int cc = t & 3, rgp = t >> 2;
    u32x4 wv[5];
    u16 cv[5];
#pragma unroll
    for (int r = 0; r < 5; ++r) {
      const int i = rgp * 5 + r;
      wv[r] = *(const u32x4*)(w_mod + (size_t)i * 3840 + j0 + 8 * cc);
      cv[r] = cvec[i];
    }
    STAMP(0, 3);
    float acc[8];
#pragma unroll
    for (int e = 0; e < 8; ++e) acc[e] = 0.f;
#pragma unroll
    for (int r = 0; r < 5; ++r) {
      const float c_ = bf2f(cv[r]);
      const float sv = rbf(c_ / (1.f + __expf(-c_)));
#pragma unroll
      for (int e = 0; e < 4; ++e) {
        acc[2 * e] += sv * bflo(wv[r][e]);
        acc[2 * e + 1] += sv * bfhi(wv[r][e]);
      }
    }
    float* red = sm;
#pragma unroll
    for (int e = 0; e < 8; ++e) red[rgp * 33 + 8 * cc + e] = acc[e];
    __syncthreads();
    {
      const int col = t >> 4, part = t & 15;
      float s = 0.f;
#pragma unroll
      for (int r = 0; r < 8; ++r) s += red[(part * 8 + r) * 33 + col];
#pragma unroll
      for (int o = 1; o < 16; o <<= 1) s += __shfl_xor(s, o);
      if (part == 0) ((float*)(ws + OFF_MOD))[j0 + col] = rbf(s);
    }
    STAMP(0, 2);
    return;
  }
  bid -= K1_MOD;
  {
    const int task = bid * 512 + t;
    if (task >= 3 * 17280) return;
    const int which = task / 17280, tk = task % 17280;
    u32x4 v[8], o[8];
    if (which < 2) {
      const int nc = tk % 216, kc = tk / 216;
      const u16* src = which == 0 ? w1 : w3;
#pragma unroll
      for (int i = 0; i < 8; ++i) v[i] = *(const u32x4*)(src + (size_t)(8 * kc + i) * F_ + 8 * nc);
      transpose8x8(v, o);
      const int nt = nc >> 1, kt = kc >> 2, g = kc & 3;
      char* dst = ws + OFF_W13F + ((size_t)((nt * 20 + kt) * 2 + which)) * 1024 + (size_t)(16 * g + 8 * (nc & 1)) * 16;
#pragma unroll
      for (int c = 0; c < 8; ++c) *(u32x4*)(dst + c * 16) = o[c];
    } else {
      const int nc = tk % 80, kc = tk / 80;
#pragma unroll
      for (int i = 0; i < 8; ++i) v[i] = *(const u32x4*)(w2 + (size_t)(8 * kc + i) * D_ + 8 * nc);
      transpose8x8(v, o);
      const int nt = nc >> 1, kt = kc >> 2, g = kc & 3;
      char* dst = ws + OFF_W2F + ((size_t)(nt * 54 + kt)) * 1024 + (size_t)(16 * g + 8 * (nc & 1)) * 16;
#pragma unroll
      for (int c = 0; c < 8; ++c) *(u32x4*)(dst + c * 16) = o[c];
    }
    STAMP(0, 2);
  }
}

constexpr int UC_STRIDE = 4608;
constexpr int UC_BYTES = 8 * UC_STRIDE;
constexpr int KR_BYTES = 384 * 16;
constexpr int K2_LDS = UC_BYTES + 2 * KR_BYTES;

DI int lag_to_ci(int lag) {
  const int r = (int)rbf((float)lag);
  if (r < 256) return r;
  if (r < 512) return 256 + ((r - 256) >> 1);
  if (r < 1024) return 384 + ((r - 512) >> 2);
  return 512 + ((r - 1024) >> 3);
}

__global__ __launch_bounds__(512) void k2_conv(const u16* __restrict__ xnT, const float* __restrict__ KD,
                                               const float* __restrict__ modv, const u16* __restrict__ Dskip,
                                               u16* __restrict__ gyT) {
  __shared__ __attribute__((aligned(16))) char sm[K2_LDS];
  char* uc = sm;
  char* krh = sm + UC_BYTES;
  char* krl = krh + KR_BYTES;
  const int d = blockIdx.x, t = threadIdx.x;
  const int w = t >> 6, lane = t & 63, c = lane & 15, g = lane >> 4;
  STAMP(1, 0);
  const float* kd = KD + (size_t)d * KD_STRIDE;
  const float sa = modv[d], ca1 = rbf(1.f + modv[640 + d]), ga = modv[1280 + d];
  const float dsk = bf2f(Dskip[d]), k0 = kd[0];
  const u32x2 xn_own = ((const u32x2*)(xnT + (size_t)d * L_))[t];

  if (t < 256) {
    const u32x4* xn4 = (const u32x4*)(xnT + (size_t)d * L_);
    u32x4 cur = xn4[t];
    u32x4 prv = {0, 0, 0, 0};
    if (t > 0) prv = xn4[t - 1];
    unsigned hv[8];
#pragma unroll
    for (int e = 0; e < 4; ++e) {
      float a = rbf(rbf(bflo(prv[e]) * ca1) + sa), b = rbf(rbf(bfhi(prv[e]) * ca1) + sa);
      hv[e] = (t > 0) ? pack2(a, b) : 0u;
      float a2 = rbf(rbf(bflo(cur[e]) * ca1) + sa), b2 = rbf(rbf(bfhi(cur[e]) * ca1) + sa);
      hv[4 + e] = pack2(a2, b2);
    }
#pragma unroll
    for (int s = 0; s < 8; ++s) {
      u32x4 o;
      if ((s & 1) == 0) {
#pragma unroll
        for (int e = 0; e < 4; ++e) o[e] = hv[s / 2 + e];
      } else {
#pragma unroll
        for (int e = 0; e < 4; ++e) o[e] = (hv[s / 2 + e] >> 16) | (hv[s / 2 + e + 1] << 16);
      }
      *(u32x4*)(uc + s * UC_STRIDE + 64 * (s & 3) + 32 * (s >> 2) + 16 * (t + 7)) = o;
    }
  } else {
    const int t2 = t - 256;
    float kv[8];
#pragma unroll
    for (int u = 0; u < 8; ++u) {
      const int lag = 2048 - (8 * t2 + u);
      kv[u] = kd[lag_to_ci(lag < 2048 ? lag : 2047)];
      if (lag == 2048) kv[u] = 0.f;
    }
    if (t2 < 56) {
      const int s = t2 / 7, m = t2 % 7;
      *(u32x4*)(uc + s * UC_STRIDE + 64 * (s & 3) + 32 * (s >> 2) + 16 * m) = u32x4{0, 0, 0, 0};
    }
    if (t2 < 120) {
      *(u32x4*)(krh + 16 * (264 + t2)) = u32x4{0, 0, 0, 0};
      *(u32x4*)(krl + 16 * (264 + t2)) = u32x4{0, 0, 0, 0};
    }
    u32x4 kh, kl;
#pragma unroll
    for (int e = 0; e < 4; ++e) {
      const float h0 = rbf(kv[2 * e]), h1 = rbf(kv[2 * e + 1]);
      kh[e] = pack2(h0, h1);
      kl[e] = pack2(kv[2 * e] - h0, kv[2 * e + 1] - h1);
    }
    const int ci = 8 + t2, R = ci >> 3, e = ci & 7;
    const int pos = 8 * R + (e ^ (R & 7));
    *(u32x4*)(krh + 16 * pos) = kh;
    *(u32x4*)(krl + 16 * pos) = kl;
  }
  __syncthreads();

  STAMP(1, 1);
  f32x4 acc[2][4];
#pragma unroll
  for (int a = 0; a < 2; ++a)
#pragma unroll
    for (int b = 0; b < 4; ++b) acc[a][b] = f32x4{0, 0, 0, 0};

  const int sg = c & 7;
  const char* ub = uc + sg * UC_STRIDE + 64 * (sg & 3) + 32 * (sg >> 2) + 16 * ((c >> 3) + g);
  int dA_lo, dA_hi, s_lo, s_hi;
  if (w < 5) { dA_lo = 6 * w; dA_hi = 6 * w + 6; s_lo = 32; s_hi = 32; }
  else if (w == 5) { dA_lo = 30; dA_hi = 32; s_lo = 32; s_hi = 40; }
  else { dA_lo = 32; dA_hi = 32; s_lo = 40 + 12 * (w - 6); s_hi = s_lo + 12; }
  for (int kp = dA_lo; kp < dA_hi; ++kp) {
    bf16x8 bfr[4];
#pragma unroll
    for (int ct = 0; ct < 4; ++ct) bfr[ct] = *(const bf16x8*)(ub + 16 * (2 * ct + 4 * kp));
    const int e = 4 * (kp & 1) + g;
    bf16x8 ah[2], al[2];
#pragma unroll
    for (int tt = 0; tt < 2; ++tt) {
      const int R = 32 - 16 * tt - c + (kp >> 1);
      const int pos = 8 * R + (e ^ (R & 7));
      ah[tt] = *(const bf16x8*)(krh + 16 * pos);
      al[tt] = *(const bf16x8*)(krl + 16 * pos);
    }
#pragma unroll
    for (int tt = 0; tt < 2; ++tt)
#pragma unroll
      for (int ct = 0; ct < 4; ++ct) {
        acc[tt][ct] = __builtin_amdgcn_mfma_f32_16x16x32_bf16(ah[tt], bfr[ct], acc[tt][ct], 0, 0, 0);
        acc[tt][ct] = __builtin_amdgcn_mfma_f32_16x16x32_bf16(al[tt], bfr[ct], acc[tt][ct], 0, 0, 0);
      }
  }
  for (int kp = s_lo; kp < s_hi; ++kp) {
    bf16x8 bfr[4];
#pragma unroll
    for (int ct = 0; ct < 4; ++ct) bfr[ct] = *(const bf16x8*)(ub + 16 * (2 * ct + 4 * kp));
    const int e = 4 * (kp & 1) + g;
    const int R = 16 - c + (kp >> 1);
    const int pos = 8 * R + (e ^ (R & 7));
    const bf16x8 ah = *(const bf16x8*)(krh + 16 * pos);
    const bf16x8 al = *(const bf16x8*)(krl + 16 * pos);
#pragma unroll
    for (int ct = 0; ct < 4; ++ct) {
      acc[1][ct] = __builtin_amdgcn_mfma_f32_16x16x32_bf16(ah, bfr[ct], acc[1][ct], 0, 0, 0);
      acc[1][ct] = __builtin_amdgcn_mfma_f32_16x16x32_bf16(al, bfr[ct], acc[1][ct], 0, 0, 0);
    }
  }
  __syncthreads();
  STAMP(1, 2);
  float* yb_ = (float*)sm;
  if (w >= 4) {
#pragma unroll
    for (int tt = 0; tt < 2; ++tt)
#pragma unroll
      for (int ct = 0; ct < 4; ++ct) *(f32x4*)(yb_ + (((w - 4) * 8 + tt * 4 + ct) * 64 + lane) * 4) = acc[tt][ct];
  }
  __syncthreads();
  if (w < 4) {
#pragma unroll
    for (int tt = 0; tt < 2; ++tt)
#pragma unroll
      for (int ct = 0; ct < 4; ++ct) acc[tt][ct] += *(const f32x4*)(yb_ + ((w * 8 + tt * 4 + ct) * 64 + lane) * 4);
  }
  __syncthreads();
  if (w < 4) {
    float* yp = yb_ + w * 2048;
#pragma unroll
    for (int tt = 0; tt < 2; ++tt)
#pragma unroll
      for (int ct = 0; ct < 4; ++ct)
#pragma unroll
        for (int j = 0; j < 4; ++j) yp[64 * (16 * tt + 4 * g + j) + 16 * ct + c] = acc[tt][ct][j];
  }
  __syncthreads();
  const float* y0 = yb_ + 4 * t;
  f32x4 s0 = *(const f32x4*)(y0);
#pragma unroll
  for (int ww = 1; ww < 4; ++ww) s0 += *(const f32x4*)(y0 + ww * 2048);
  float r[4];
#pragma unroll
  for (int i = 0; i < 4; ++i) {
    const float xn = (i & 1) ? bfhi(xn_own[i >> 1]) : bflo(xn_own[i >> 1]);
    const float hh = rbf(rbf(xn * ca1) + sa);
    const float yb = rbf(s0[i] + k0 * hh);
    const float sk = rbf(hh * dsk);
    const float sy = rbf(yb + sk);
    r[i] = ga * sy;
  }
  ((u32x2*)(gyT + (size_t)d * L_))[t] = u32x2{pack2(r[0], r[1]), pack2(r[2], r[3])};
  STAMP(1, 3);
}

__global__ __launch_bounds__(320) void k3_ln2(const u16* __restrict__ gyT, const u16* __restrict__ x,
                                              const float* __restrict__ modv, u16* __restrict__ x1,
                                              char* __restrict__ h2F) {
  __shared__ float red[16 * 81];
  __shared__ float stat[32];
  const int t = threadIdx.x;
  const int l0 = ((blockIdx.x & 7) * 16 + (blockIdx.x >> 3)) * 16;
  STAMP(2, 0);
  const int cg = t & 3, ch = t >> 2;
  u32x2 v[8];
#pragma unroll
  for (int i = 0; i < 8; ++i) v[i] = *(const u32x2*)(gyT + (size_t)(8 * ch + i) * L_ + l0 + 4 * cg);
  u32x4 xr[4];
#pragma unroll
  for (int c = 0; c < 4; ++c) xr[c] = *(const u32x4*)(x + (size_t)(l0 + 4 * cg + c) * D_ + 8 * ch);
  const f32x4 sb0 = *(const f32x4*)(modv + 3 * 640 + 8 * ch), sb1 = *(const f32x4*)(modv + 3 * 640 + 8 * ch + 4);
  const f32x4 cb0 = *(const f32x4*)(modv + 4 * 640 + 8 * ch), cb1 = *(const f32x4*)(modv + 4 * 640 + 8 * ch + 4);
  u32x4 o[4];
#pragma unroll
  for (int dd = 0; dd < 2; ++dd)
#pragma unroll
    for (int p = 0; p < 4; ++p) {
      unsigned a = v[2 * p][dd], b = v[2 * p + 1][dd];
      o[2 * dd][p] = pack2(bflo(xr[2 * dd][p]) + bflo(a), bfhi(xr[2 * dd][p]) + bflo(b));
      o[2 * dd + 1][p] = pack2(bflo(xr[2 * dd + 1][p]) + bfhi(a), bfhi(xr[2 * dd + 1][p]) + bfhi(b));
    }
#pragma unroll
  for (int c = 0; c < 4; ++c) {
    float s = 0.f;
#pragma unroll
    for (int e = 0; e < 4; ++e) s += bflo(o[c][e]) + bfhi(o[c][e]);
    red[(4 * cg + c) * 81 + ch] = s;
  }
  __syncthreads();
  if (t < 256) {
    const int row = t >> 4, part = t & 15;
    float s = 0.f;
#pragma unroll
    for (int e = 0; e < 5; ++e) s += red[row * 81 + part * 5 + e];
#pragma unroll
    for (int of = 1; of < 16; of <<= 1) s += __shfl_xor(s, of);
    if (part == 0) stat[row] = s * (1.f / 640.f);
  }
  __syncthreads();
  STAMP(2, 1);
  float mean[4];
#pragma unroll
  for (int c = 0; c < 4; ++c) {
    mean[c] = stat[4 * cg + c];
    float s = 0.f;
#pragma unroll
    for (int e = 0; e < 4; ++e) {
      float a = bflo(o[c][e]) - mean[c], b = bfhi(o[c][e]) - mean[c];
      s += a * a + b * b;
    }
    red[(4 * cg + c) * 81 + ch] = s;
  }
  __syncthreads();
  if (t < 256) {
    const int row = t >> 4, part = t & 15;
    float s = 0.f;
#pragma unroll
    for (int e = 0; e < 5; ++e) s += red[row * 81 + part * 5 + e];
#pragma unroll
    for (int of = 1; of < 16; of <<= 1) s += __shfl_xor(s, of);
    if (part == 0) stat[16 + row] = s * (1.f / 640.f);
  }
  __syncthreads();
  STAMP(2, 2);
  float sbv[8] = {sb0[0], sb0[1], sb0[2], sb0[3], sb1[0], sb1[1], sb1[2], sb1[3]};
  float cbv[8] = {cb0[0], cb0[1], cb0[2], cb0[3], cb1[0], cb1[1], cb1[2], cb1[3]};
#pragma unroll
  for (int i = 0; i < 8; ++i) cbv[i] = rbf(1.f + cbv[i]);
  const int kt = ch >> 2, gq = ch & 3;
#pragma unroll
  for (int c = 0; c < 4; ++c) {
    const int m = l0 + 4 * cg + c;
    const float mb = rbf(mean[c]);
    const float den = rbf(sqrtf(rbf(rbf(stat[16 + 4 * cg + c]) + 1e-5f)));
    const float rden = frcp(den);
    u32x4 hv;
#pragma unroll
    for (int e = 0; e < 4; ++e) {
      float a = rbf(fdiv(rbf(bflo(o[c][e]) - mb), den, rden)), b = rbf(fdiv(rbf(bfhi(o[c][e]) - mb), den, rden));
      a = rbf(a * cbv[2 * e]) + sbv[2 * e];
      b = rbf(b * cbv[2 * e + 1]) + sbv[2 * e + 1];
      hv[e] = pack2(a, b);
    }
    *(u32x4*)(h2F + ((size_t)((m >> 4) * 20 + kt)) * 1024 + (size_t)((m & 15) + 16 * gq) * 16) = hv;
    *(u32x4*)(x1 + (size_t)m * D_ + 8 * ch) = o[c];
  }
  STAMP(2, 3);
}

constexpr int G1_SLOT = 24576, G1_PF = 4;

__global__ __launch_bounds__(512) void k4_gemm1(const char* __restrict__ h2F, const char* __restrict__ W13F,
                                                char* __restrict__ hidF) {
  __shared__ __attribute__((aligned(16))) char sm[3 * G1_SLOT];
  const int t = threadIdx.x, w = t >> 6, lane = t & 63;
  const int xcd = blockIdx.x & 7, idx = blockIdx.x >> 3;
  const int nq = xcd >> 1, nstart = 7 * nq, ncnt = (nq == 3) ? 6 : 7;
  if (idx >= 4 * ncnt) return;
  const int bm = 4 * (xcd & 1) + (idx & 3), bn = nstart + (idx >> 2);
  const int wm = w >> 1, wn = w & 1;
  STAMP(3, 0);
  const char* hsrc = h2F + ((size_t)((16 * bm + 2 * w) * 20)) * 1024 + lane * 16;
  const char* wsrc = W13F + ((size_t)((4 * bn + (w >> 1)) * 20)) * 2048 + (w & 1) * 1024 + lane * 16;
  f32x4 acc[2][2][4];
#pragma unroll
  for (int a = 0; a < 2; ++a)
#pragma unroll
    for (int b = 0; b < 2; ++b)
#pragma unroll
      for (int cI = 0; cI < 4; ++cI) acc[a][b][cI] = f32x4{0, 0, 0, 0};
  u32x4 pf[G1_PF][3];
#define G1_LOAD(R, S) do { pf[R][0] = *(const u32x4*)(hsrc + (size_t)(S) * 1024); \
    pf[R][1] = *(const u32x4*)(hsrc + (size_t)(S) * 1024 + 20 * 1024); \
    pf[R][2] = *(const u32x4*)(wsrc + (size_t)(S) * 2048); } while (0)
#define G1_WRITE(R, S) do { char* nb_ = sm + ((S) % 3) * G1_SLOT; \
    *(u32x4*)(nb_ + (2 * w) * 1024 + lane * 16) = pf[R][0]; \
    *(u32x4*)(nb_ + (2 * w + 1) * 1024 + lane * 16) = pf[R][1]; \
    *(u32x4*)(nb_ + 16384 + w * 1024 + lane * 16) = pf[R][2]; } while (0)
#define G1_READ(B, S) do { const char* b_ = sm + ((S) % 3) * G1_SLOT; \
    _Pragma("unroll") for (int i = 0; i < 4; ++i) hb[B][i] = *(const bf16x8*)(b_ + (4 * wm + i) * 1024 + lane * 16); \
    _Pragma("unroll") for (int a = 0; a < 2; ++a) _Pragma("unroll") for (int b = 0; b < 2; ++b) \
      wa[B][a][b] = *(const bf16x8*)(b_ + 16384 + ((2 * wn + a) * 2 + b) * 1024 + lane * 16); } while (0)
  bf16x8 hb[2][4], wa[2][2][2];
#pragma unroll
  for (int s = 0; s < G1_PF; ++s) G1_LOAD(s, s);
  G1_WRITE(0, 0); G1_LOAD(0, G1_PF);
  G1_WRITE(1, 1); G1_LOAD(1, G1_PF + 1);
  __syncthreads();
  G1_READ(0, 0);
#pragma unroll
  for (int s = 0; s < 20; ++s) {
    if (s + 2 < 20) {
      G1_WRITE((s + 2) % G1_PF, s + 2);
      if (s + 2 + G1_PF < 20) G1_LOAD((s + 2) % G1_PF, s + 2 + G1_PF);
    }
    if (s + 1 < 20) G1_READ((s + 1) & 1, s + 1);
#pragma unroll
    for (int a = 0; a < 2; ++a)
#pragma unroll
      for (int b = 0; b < 2; ++b)
#pragma unroll
        for (int i = 0; i < 4; ++i)
          acc[a][b][i] = __builtin_amdgcn_mfma_f32_16x16x32_bf16(wa[s & 1][a][b], hb[s & 1][i], acc[a][b][i], 0, 0, 0);
    if (s + 2 < 20) __syncthreads();
  }
  STAMP(3, 1);
  const int cl = lane & 15, g = lane >> 4;
#pragma unroll
  for (int a = 0; a < 2; ++a) {
    const int nt = 4 * bn + 2 * wn + a;
    const int kt2 = nt >> 1;
    const int slot = 16 * (2 * (nt & 1) + (g >> 1)) + cl;
#pragma unroll
    for (int i = 0; i < 4; ++i) {
      const int mt = 16 * bm + 4 * wm + i;
      float hv[4];
#pragma unroll
      for (int j = 0; j < 4; ++j) {
        const float gv = rbf(acc[a][0][i][j]), uv = rbf(acc[a][1][i][j]);
        const float dn = 1.f + __expf(-gv);
        const float sv = rbf(fdiv(gv, dn, frcp(dn)));
        hv[j] = sv * uv;
      }
      u32x2 pv = {pack2(hv[0], hv[1]), pack2(hv[2], hv[3])};
      *(u32x2*)(hidF + ((size_t)(mt * 54 + kt2)) * 1024 + slot * 16 + (g & 1) * 8) = pv;
    }
  }
  STAMP(3, 2);
}

__global__ __launch_bounds__(512) void k5_gemm2(const char* __restrict__ hidF, const char* __restrict__ W2F,
                                                const u16* __restrict__ x1, const float* __restrict__ modv,
                                                u16* __restrict__ out) {
  __shared__ __attribute__((aligned(16))) float sm[8 * 20 * 256];
  const int t = threadIdx.x, w = t >> 6, lane = t & 63;
  const int bn = (blockIdx.x >> 3) & 7, bm = 4 * (blockIdx.x & 7) + (blockIdx.x >> 6);
  const int k_lo = (w < 6) ? 7 * w : 42 + 6 * (w - 6);
  const int nk = (w < 6) ? 7 : 6;
  f32x4 acc[5][4];
#pragma unroll
  for (int a = 0; a < 5; ++a)
#pragma unroll
    for (int b = 0; b < 4; ++b) acc[a][b] = f32x4{0, 0, 0, 0};
  STAMP(4, 0);
  const char* hp = hidF + ((size_t)(4 * bm) * 54 + k_lo) * 1024 + lane * 16;
  const char* wp = W2F + ((size_t)(5 * bn) * 54 + k_lo) * 1024 + lane * 16;
  bf16x8 hb[3][4], wa[3][5];
#pragma unroll
  for (int p = 0; p < 2; ++p) {
#pragma unroll
    for (int i = 0; i < 4; ++i) hb[p][i] = *(const bf16x8*)(hp + (size_t)(i * 54 + p) * 1024);
#pragma unroll
    for (int i = 0; i < 5; ++i) wa[p][i] = *(const bf16x8*)(wp + (size_t)(i * 54 + p) * 1024);
  }
  const int c = lane & 15, g = lane >> 4;
  const int t_lo = (w < 4) ? 3 * w : 12 + 2 * (w - 4);
  const int t_n = (w < 4) ? 3 : 2;
  u32x2 xv[3];
  f32x4 gbv[3];
#pragma unroll
  for (int q = 0; q < 3; ++q) {
    const int tile = (q < t_n) ? t_lo + q : t_lo;
    const int a = tile >> 2, b = tile & 3;
    const int m = 16 * (4 * bm + b) + c;
    const int n = 16 * (5 * bn + a) + 4 * g;
    xv[q] = *(const u32x2*)(x1 + (size_t)m * D_ + n);
    gbv[q] = *(const f32x4*)(modv + 5 * 640 + n);
  }
#pragma unroll
  for (int k = 0; k < 7; ++k) {
    if (k < nk) {
      if (k + 2 < nk) {
#pragma unroll
        for (int i = 0; i < 4; ++i) hb[(k + 2) % 3][i] = *(const bf16x8*)(hp + (size_t)(i * 54 + k + 2) * 1024);
#pragma unroll
        for (int i = 0; i < 5; ++i) wa[(k + 2) % 3][i] = *(const bf16x8*)(wp + (size_t)(i * 54 + k + 2) * 1024);
      }
#pragma unroll
      for (int a = 0; a < 5; ++a)
#pragma unroll
        for (int b = 0; b < 4; ++b)
          acc[a][b] = __builtin_amdgcn_mfma_f32_16x16x32_bf16(wa[k % 3][a], hb[k % 3][b], acc[a][b], 0, 0, 0);
    }
  }
  STAMP(4, 1);
#pragma unroll
  for (int a = 0; a < 5; ++a)
#pragma unroll
    for (int b = 0; b < 4; ++b) *(f32x4*)(sm + ((w * 20 + a * 4 + b) * 64 + lane) * 4) = acc[a][b];
  __syncthreads();
  STAMP(4, 2);
#pragma unroll
  for (int q = 0; q < 3; ++q) {
    if (q < t_n) {
      const int tile = t_lo + q;
      const int a = tile >> 2, b = tile & 3;
      const int m = 16 * (4 * bm + b) + c;
      const int n = 16 * (5 * bn + a) + 4 * g;
      f32x4 s = *(const f32x4*)(sm + ((0 * 20 + tile) * 64 + lane) * 4);
#pragma unroll
      for (int ww = 1; ww < 8; ++ww) s += *(const f32x4*)(sm + ((ww * 20 + tile) * 64 + lane) * 4);
      float r0 = bflo(xv[q][0]) + rbf(gbv[q][0] * rbf(s[0]));
      float r1 = bfhi(xv[q][0]) + rbf(gbv[q][1] * rbf(s[1]));
      float r2 = bflo(xv[q][1]) + rbf(gbv[q][2] * rbf(s[2]));
      float r3 = bfhi(xv[q][1]) + rbf(gbv[q][3] * rbf(s[3]));
      u32x2 ov = {pack2(r0, r1), pack2(r2, r3)};
      *(u32x2*)(out + (size_t)m * D_ + n) = ov;
    }
  }
  STAMP(4, 3);
}

extern "C" void kernel_launch(void* const* d_in, const int* in_sizes, int n_in, void* d_out, int out_size,
                              void* d_ws, size_t ws_size, hipStream_t stream) {
  const u16* x = (const u16*)d_in[0];
  const u16* A_re = (const u16*)d_in[1];
  const u16* A_im = (const u16*)d_in[2];
  const u16* B_re = (const u16*)d_in[3];
  const u16* B_im = (const u16*)d_in[4];
  const u16* C_re = (const u16*)d_in[5];
  const u16* C_im = (const u16*)d_in[6];
  const u16* log_dt = (const u16*)d_in[7];
  const u16* Dskip = (const u16*)d_in[8];
  const u16* w1 = (const u16*)d_in[10];
  const u16* w3 = (const u16*)d_in[11];
  const u16* w2 = (const u16*)d_in[12];
  const u16* cvec = (const u16*)d_in[13];
  const u16* w_mod = (const u16*)d_in[14];
  char* ws = (char*)d_ws;
  u16* out = (u16*)d_out;

  k1_prep<<<K1_KGEN + K1_LN + K1_MOD + K1_RT, 512, 0, stream>>>(x, A_re, A_im, B_re, B_im, C_re, C_im, log_dt,
                                                                 w1, w3, w2, cvec, w_mod, ws);
  k2_conv<<<640, 512, 0, stream>>>((const u16*)(ws + OFF_XNT), (const float*)(ws + OFF_KD),
                                   (const float*)(ws + OFF_MOD), Dskip, (u16*)(ws + OFF_GYT));
  k3_ln2<<<128, 320, 0, stream>>>((const u16*)(ws + OFF_GYT), x, (const float*)(ws + OFF_MOD), (u16*)(ws + OFF_X1),
                                  ws + OFF_H2F);
  k4_gemm1<<<8 * 28, 512, 0, stream>>>(ws + OFF_H2F, ws + OFF_W13F, ws + OFF_HIDF);
  k5_gemm2<<<32 * 8, 512, 0, stream>>>(ws + OFF_HIDF, ws + OFF_W2F, (const u16*)(ws + OFF_X1),
                                       (const float*)(ws + OFF_MOD), out);
}
```

```cpp
#include <hip/hip_runtime.h>
#include <hip/hip_bf16.h>

typedef __attribute__((ext_vector_type(8))) short bf16x8;
typedef __attribute__((ext_vector_type(4))) float f32x4;
typedef __attribute__((ext_vector_type(4))) unsigned u32x4;
typedef __attribute__((ext_vector_type(2))) unsigned u32x2;
typedef unsigned short u16;

#define DI __device__ __forceinline__
#define NT_STORE(P, V) (*(P) = (V))
#define STAMP(K, I)

constexpr int L_ = 2048, D_ = 640, N_ = 128, F_ = 1728;

constexpr size_t OFF_MOD = 0;
constexpr size_t OFF_K0 = 16384;
constexpr size_t OFF_XNT = 32768;
constexpr size_t SZ_ACT = (size_t)D_ * L_ * 2;
constexpr int KD_STRIDE = 648;
constexpr size_t OFF_KD = OFF_XNT + SZ_ACT;
constexpr size_t OFF_GYT = OFF_KD + (size_t)D_ * KD_STRIDE * 4;
constexpr size_t OFF_X1 = OFF_GYT + SZ_ACT;
constexpr size_t OFF_H2F = OFF_X1 + SZ_ACT;
constexpr size_t OFF_W13F = OFF_H2F + SZ_ACT;
constexpr size_t SZ_W = (size_t)D_ * F_ * 2;
constexpr size_t OFF_W2F = OFF_W13F + 2 * SZ_W;
constexpr size_t OFF_HIDF = OFF_W2F + SZ_W;

DI float bf2f(unsigned v) { return __uint_as_float(v << 16); }
DI float bflo(unsigned v) { return __uint_as_float(v << 16); }
DI float bfhi(unsigned v) { return __uint_as_float(v & 0xffff0000u); }
DI float rbf(float x) { return (float)(__bf16)x; }
DI unsigned f2bf(float x) { __bf16 b = (__bf16)x; return (unsigned)__builtin_bit_cast(u16, b); }
DI unsigned pack2(float lo, float hi) { return f2bf(lo) | (f2bf(hi) << 16); }
DI float frcp(float x) { return __builtin_amdgcn_rcpf(x); }
DI float fdiv(float a, float b, float rb) { float q = a * rb; float r = fmaf(-q, b, a); return fmaf(r, rb, q); }

DI void transpose8x8(const u32x4* in, u32x4* out) {
#pragma unroll
  for (int d = 0; d < 4; ++d) {
#pragma unroll
    for (int p = 0; p < 4; ++p) {
      unsigned a = in[2 * p][d], b = in[2 * p + 1][d];
      out[2 * d][p] = (a & 0xffffu) | (b << 16);
      out[2 * d + 1][p] = (a >> 16) | (b & 0xffff0000u);
    }
  }
}

typedef __attribute__((ext_vector_type(2))) float c32;
DI c32 cexp_l(float ar, float ai, float l) {
  const float mag = __expf(ar * l);
  float rev = (ai * l) * 0.15915494309189535f;
  rev = rev - floorf(rev);
  c32 cs = {__builtin_amdgcn_cosf(rev), __builtin_amdgcn_sinf(rev)};
  return cs * mag;
}
DI c32 cmul(c32 a, c32 b) {
  c32 bs = {-b.y, b.x};
  return a.xx * b + a.yy * bs;
}

constexpr int K1_KGEN = 640, K1_LN = 256, K1_MOD = 120, K1_RT = 102;

__global__ __launch_bounds__(512) void k1_prep(
    const u16* __restrict__ x, const u16* __restrict__ A_re, const u16* __restrict__ A_im,
    const u16* __restrict__ B_re, const u16* __restrict__ B_im, const u16* __restrict__ C_re,
    const u16* __restrict__ C_im, const u16* __restrict__ log_dt, const u16* __restrict__ w1,
    const u16* __restrict__ w3, const u16* __restrict__ w2, const u16* __restrict__ cvec,
    const u16* __restrict__ w_mod, char* __restrict__ ws) {
  __shared__ __attribute__((aligned(16))) float sm[8 * 4 * 256 + 1536];
  const int t = threadIdx.x;
  int bid = blockIdx.x;
  STAMP(0, 0);
  if (bid < K1_KGEN) {
    const int d = bid;
    const int w = t >> 6, lane = t & 63, i = lane & 15, kk = lane >> 4;
    float* prm = sm + 8192;
    if (t < 128) {
      const int n = d * N_ + t;
      const float dt = rbf(__expf(bf2f(log_dt[d])));
      const float ar = dt * bf2f(A_re[n]), ai = dt * bf2f(A_im[n]);
      const c32 db = {dt * bf2f(B_re[n]), dt * bf2f(B_im[n])};
      const c32 cc = {bf2f(C_re[n]), bf2f(C_im[n])};
      const c32 c0 = cmul(cc, db);
      c32 gg = cexp_l(ar, ai, 256.f);
      const c32 c1 = cmul(gg, c0);
      gg = cmul(gg, gg);
      const c32 c2 = cmul(gg, c0);
      gg = cmul(gg, gg);
      const c32 c3 = cmul(gg, c0);
      *(f32x4*)(prm + t * 12) = f32x4{ar, ai, c0.x, c0.y};
      *(f32x4*)(prm + t * 12 + 4) = f32x4{c1.x, c1.y, c2.x, c2.y};
      *(f32x4*)(prm + t * 12 + 8) = f32x4{c3.x, c3.y, 0.f, 0.f};
    }
    __syncthreads();
    STAMP(0, 3);
    f32x4 acc0 = {0, 0, 0, 0}, acc1 = {0, 0, 0, 0}, acc2 = {0, 0, 0, 0}, acc3 = {0, 0, 0, 0};
    const float fi = (float)i;
#pragma unroll
    for (int s = 0; s < 4; ++s) {
      const int n = 16 * w + 4 * s + kk;
      const f32x4 p0 = *(const f32x4*)(prm + n * 12), p1 = *(const f32x4*)(prm + n * 12 + 4), p2 = *(const f32x4*)(prm + n * 12 + 8);
      c32 e = cexp_l(p0[0], p0[1], fi);
      c32 xx = cexp_l(p0[0], p0[1], 16.f * fi);
      c32 pp;
      pp = cmul(xx, c32{p0[2], p0[3]});
      acc0 = __builtin_amdgcn_mfma_f32_16x16x4f32(pp.x, e.x, acc0, 0, 0, 0);
      acc0 = __builtin_amdgcn_mfma_f32_16x16x4f32(-pp.y, e.y, acc0, 0, 0, 0);
      e = cmul(e, e); xx = cmul(xx, xx);
      pp = cmul(xx, c32{p1[0], p1[1]});
      acc1 = __builtin_amdgcn_mfma_f32_16x16x4f32(pp.x, e.x, acc1, 0, 0, 0);
      acc1 = __builtin_amdgcn_mfma_f32_16x16x4f32(-pp.y, e.y, acc1, 0, 0, 0);
      e = cmul(e, e); xx = cmul(xx, xx);
      pp = cmul(xx, c32{p1[2], p1[3]});
      acc2 = __builtin_amdgcn_mfma_f32_16x16x4f32(pp.x, e.x, acc2, 0, 0, 0);
      acc2 = __builtin_amdgcn_mfma_f32_16x16x4f32(-pp.y, e.y, acc2, 0, 0, 0);
      e = cmul(e, e); xx = cmul(xx, xx);
      pp = cmul(xx, c32{p2[0], p2[1]});
      acc3 = __builtin_amdgcn_mfma_f32_16x16x4f32(pp.x, e.x, acc3, 0, 0, 0);
      acc3 = __builtin_amdgcn_mfma_f32_16x16x4f32(-pp.y, e.y, acc3, 0, 0, 0);
    }
    STAMP(0, 1);
    float* red = sm;
#pragma unroll
    for (int j = 0; j < 4; ++j) {
      const int idx = 16 * (4 * kk + j) + i;
      red[(w * 4 + 0) * 256 + idx] = acc0[j];
      red[(w * 4 + 1) * 256 + idx] = acc1[j];
      red[(w * 4 + 2) * 256 + idx] = acc2[j];
      red[(w * 4 + 3) * 256 + idx] = acc3[j];
    }
    __syncthreads();
    STAMP(0, 4);
    float* kd = (float*)(ws + OFF_KD) + (size_t)d * KD_STRIDE;
    for (int ci = t; ci < 641; ci += 512) {
      int tile, idx;
      if (ci < 256) { tile = 0; idx = ci; }
      else if (ci < 384) { tile = 1; idx = ci - 256; }
      else if (ci < 512) { tile = 2; idx = ci - 384; }
      else { tile = 3; idx = ci - 512; }
      float v = 0.f;
#pragma unroll
      for (int ww = 0; ww < 8; ++ww) v += red[(ww * 4 + tile) * 256 + idx];
      kd[ci] = v;
    }
    STAMP(0, 2);
    return;
  }
  bid -= K1_KGEN;
  if (bid < K1_LN) {
    const int l0 = ((bid & 7) * 32 + (bid >> 3)) * 8;
    const int w = t >> 6, lane = t & 63;
    const unsigned* xr = (const unsigned*)(x + (size_t)(l0 + w) * D_);
    unsigned v[5];
#pragma unroll
    for (int j = 0; j < 5; ++j) v[j] = xr[lane + 64 * j];
    float s = 0.f;
#pragma unroll
    for (int j = 0; j < 5; ++j) s += bflo(v[j]) + bfhi(v[j]);
    STAMP(0, 3);
#pragma unroll
    for (int o = 1; o < 64; o <<= 1) s += __shfl_xor(s, o);
    const float mean = s * (1.f / 640.f);
    float q = 0.f;
#pragma unroll
    for (int j = 0; j < 5; ++j) {
      float a = bflo(v[j]) - mean, b = bfhi(v[j]) - mean;
      q += a * a + b * b;
    }
#pragma unroll
    for (int o = 1; o < 64; o <<= 1) q += __shfl_xor(q, o);
    const float mb = rbf(mean);
    const float den = rbf(sqrtf(rbf(rbf(q * (1.f / 640.f)) + 1e-5f)));
    const float rden = frcp(den);
    unsigned* sn = (unsigned*)sm;
#pragma unroll
    for (int j = 0; j < 5; ++j) {
      float a = fdiv(rbf(bflo(v[j]) - mb), den, rden), b = fdiv(rbf(bfhi(v[j]) - mb), den, rden);
      sn[w * 320 + lane + 64 * j] = pack2(a, b);
    }
    __syncthreads();
    STAMP(0, 4);
    const u16* sn16 = (const u16*)sn;
    u16* xnT = (u16*)(ws + OFF_XNT);
    for (int dd = t; dd < 640; dd += 512) {
      u32x4 a;
#pragma unroll
      for (int e = 0; e < 4; ++e)
        a[e] = (unsigned)sn16[(2 * e) * 640 + dd] | ((unsigned)sn16[(2 * e + 1) * 640 + dd] << 16);
      *(u32x4*)(xnT + (size_t)dd * L_ + l0) = a;
    }
    STAMP(0, 2);
    return;
  }
  bid -= K1_LN;
  if (bid < K1_MOD) {
    const int j0 = bid * 32;
    const int cc = t & 3, rgp = t >> 2;
    u32x4 wv[5];
    u16 cv[5];
#pragma unroll
    for (int r = 0; r < 5; ++r) {
      const int i = rgp * 5 + r;
      wv[r] = *(const u32x4*)(w_mod + (size_t)i * 3840 + j0 + 8 * cc);
      cv[r] = cvec[i];
    }
    STAMP(0, 3);
    float acc[8];
#pragma unroll
    for (int e = 0; e < 8; ++e) acc[e] = 0.f;
#pragma unroll
    for (int r = 0; r < 5; ++r) {
      const float c_ = bf2f(cv[r]);
      const float sv = rbf(c_ / (1.f + __expf(-c_)));
#pragma unroll
      for (int e = 0; e < 4; ++e) {
        acc[2 * e] += sv * bflo(wv[r][e]);
        acc[2 * e + 1] += sv * bfhi(wv[r][e]);
      }
    }
    float* red = sm;
#pragma unroll
    for (int e = 0; e < 8; ++e) red[rgp * 33 + 8 * cc + e] = acc[e];
    __syncthreads();
    {
      const int col = t >> 4, part = t & 15;
      float s = 0.f;
#pragma unroll
      for (int r = 0; r < 8; ++r) s += red[(part * 8 + r) * 33 + col];
#pragma unroll
      for (int o = 1; o < 16; o <<= 1) s += __shfl_xor(s, o);
      if (part == 0) ((float*)(ws + OFF_MOD))[j0 + col] = rbf(s);
    }
    STAMP(0, 2);
    return;
  }
  bid -= K1_MOD;
  {
    const int task = bid * 512 + t;
    if (task >= 3 * 17280) return;
    const int which = task / 17280, tk = task % 17280;
    u32x4 v[8], o[8];
    if (which < 2) {
      const int nc = tk % 216, kc = tk / 216;
      const u16* src = which == 0 ? w1 : w3;
#pragma unroll
      for (int i = 0; i < 8; ++i) v[i] = *(const u32x4*)(src + (size_t)(8 * kc + i) * F_ + 8 * nc);
      transpose8x8(v, o);
      const int nt = nc >> 1, kt = kc >> 2, g = kc & 3;
      char* dst = ws + OFF_W13F + ((size_t)((nt * 20 + kt) * 2 + which)) * 1024 + (size_t)(16 * g + 8 * (nc & 1)) * 16;
#pragma unroll
      for (int c = 0; c < 8; ++c) NT_STORE((u32x4*)(dst + c * 16), o[c]);
    } else {
      const int nc = tk % 80, kc = tk / 80;
#pragma unroll
      for (int i = 0; i < 8; ++i) v[i] = *(const u32x4*)(w2 + (size_t)(8 * kc + i) * D_ + 8 * nc);
      transpose8x8(v, o);
      const int nt = nc >> 1, kt = kc >> 2, g = kc & 3;
      char* dst = ws + OFF_W2F + ((size_t)(nt * 54 + kt)) * 1024 + (size_t)(16 * g + 8 * (nc & 1)) * 16;
#pragma unroll
      for (int c = 0; c < 8; ++c) NT_STORE((u32x4*)(dst + c * 16), o[c]);
    }
    STAMP(0, 2);
  }
}

constexpr int UC_STRIDE = 4608;
constexpr int UC_BYTES = 8 * UC_STRIDE;
constexpr int KR_BYTES = 384 * 16;
constexpr int K2_LDS = UC_BYTES + 2 * KR_BYTES;

DI int lag_to_ci(int lag) {
  const int r = (int)rbf((float)lag);
  if (r < 256) return r;
  if (r < 512) return 256 + ((r - 256) >> 1);
  if (r < 1024) return 384 + ((r - 512) >> 2);
  return 512 + ((r - 1024) >> 3);
}

__global__ __launch_bounds__(512) void k2_conv(const u16* __restrict__ xnT, const float* __restrict__ KD,
                                               const float* __restrict__ modv, const u16* __restrict__ Dskip,
                                               u16* __restrict__ gyT) {
  __shared__ __attribute__((aligned(16))) char sm[K2_LDS];
  char* uc = sm;
  char* krh = sm + UC_BYTES;
  char* krl = krh + KR_BYTES;
  const int d = blockIdx.x, t = threadIdx.x;
  const int w = t >> 6, lane = t & 63, c = lane & 15, g = lane >> 4;
  STAMP(1, 0);
  const float* kd = KD + (size_t)d * KD_STRIDE;
  const float sa = modv[d], ca1 = rbf(1.f + modv[640 + d]), ga = modv[1280 + d];
  const float dsk = bf2f(Dskip[d]), k0 = kd[0];
  const u32x2 xn_own = ((const u32x2*)(xnT + (size_t)d * L_))[t];

  if (t < 256) {
    const u32x4* xn4 = (const u32x4*)(xnT + (size_t)d * L_);
    u32x4 cur = xn4[t];
    u32x4 prv = {0, 0, 0, 0};
    if (t > 0) prv = xn4[t - 1];
    unsigned hv[8];
#pragma unroll
    for (int e = 0; e < 4; ++e) {
      float a = rbf(rbf(bflo(prv[e]) * ca1) + sa), b = rbf(rbf(bfhi(prv[e]) * ca1) + sa);
      hv[e] = (t > 0) ? pack2(a, b) : 0u;
      float a2 = rbf(rbf(bflo(cur[e]) * ca1) + sa), b2 = rbf(rbf(bfhi(cur[e]) * ca1) + sa);
      hv[4 + e] = pack2(a2, b2);
    }
#pragma unroll
    for (int s = 0; s < 8; ++s) {
      u32x4 o;
      if ((s & 1) == 0) {
#pragma unroll
        for (int e = 0; e < 4; ++e) o[e] = hv[s / 2 + e];
      } else {
#pragma unroll
        for (int e = 0; e < 4; ++e) o[e] = (hv[s / 2 + e] >> 16) | (hv[s / 2 + e + 1] << 16);
      }
      *(u32x4*)(uc + s * UC_STRIDE + 64 * (s & 3) + 32 * (s >> 2) + 16 * (t + 7)) = o;
    }
  } else {
    const int t2 = t - 256;
    float kv[8];
#pragma unroll
    for (int u = 0; u < 8; ++u) {
      const int lag = 2048 - (8 * t2 + u);
      kv[u] = kd[lag_to_ci(lag < 2048 ? lag : 2047)];
      if (lag == 2048) kv[u] = 0.f;
    }
    if (t2 < 56) {
      const int s = t2 / 7, m = t2 % 7;
      *(u32x4*)(uc + s * UC_STRIDE + 64 * (s & 3) + 32 * (s >> 2) + 16 * m) = u32x4{0, 0, 0, 0};
    }
    if (t2 < 120) {
      *(u32x4*)(krh + 16 * (264 + t2)) = u32x4{0, 0, 0, 0};
      *(u32x4*)(krl + 16 * (264 + t2)) = u32x4{0, 0, 0, 0};
    }
    u32x4 kh, kl;
#pragma unroll
    for (int e = 0; e < 4; ++e) {
      const float h0 = rbf(kv[2 * e]), h1 = rbf(kv[2 * e + 1]);
      kh[e] = pack2(h0, h1);
      kl[e] = pack2(kv[2 * e] - h0, kv[2 * e + 1] - h1);
    }
    const int ci = 8 + t2, R = ci >> 3, e = ci & 7;
    const int pos = 8 * R + (e ^ (R & 7));
    *(u32x4*)(krh + 16 * pos) = kh;
    *(u32x4*)(krl + 16 * pos) = kl;
  }
  __syncthreads();

  STAMP(1, 1);
  f32x4 acc[2][4];
#pragma unroll
  for (int a = 0; a < 2; ++a)
#pragma unroll
    for (int b = 0; b < 4; ++b) acc[a][b] = f32x4{0, 0, 0, 0};

  const int sg = c & 7;
  const char* ub = uc + sg * UC_STRIDE + 64 * (sg & 3) + 32 * (sg >> 2) + 16 * ((c >> 3) + g);
  int dA_lo, dA_hi, s_lo, s_hi;
  if (w < 5) { dA_lo = 6 * w; dA_hi = 6 * w + 6; s_lo = 32; s_hi = 32; }
  else if (w == 5) { dA_lo = 30; dA_hi = 32; s_lo = 32; s_hi = 40; }
  else { dA_lo = 32; dA_hi = 32; s_lo = 40 + 12 * (w - 6); s_hi = s_lo + 12; }
  for (int kp = dA_lo; kp < dA_hi; ++kp) {
    bf16x8 bfr[4];
#pragma unroll
    for (int ct = 0; ct < 4; ++ct) bfr[ct] = *(const bf16x8*)(ub + 16 * (2 * ct + 4 * kp));
    const int e = 4 * (kp & 1) + g;
    bf16x8 ah[2], al[2];
#pragma unroll
    for (int tt = 0; tt < 2; ++tt) {
      const int R = 32 - 16 * tt - c + (kp >> 1);
      const int pos = 8 * R + (e ^ (R & 7));
      ah[tt] = *(const bf16x8*)(krh + 16 * pos);
      al[tt] = *(const bf16x8*)(krl + 16 * pos);
    }
#pragma unroll
    for (int tt = 0; tt < 2; ++tt)
#pragma unroll
      for (int ct = 0; ct < 4; ++ct) {
        acc[tt][ct] = __builtin_amdgcn_mfma_f32_16x16x32_bf16(ah[tt], bfr[ct], acc[tt][ct], 0, 0, 0);
        acc[tt][ct] = __builtin_amdgcn_mfma_f32_16x16x32_bf16(al[tt], bfr[ct], acc[tt][ct], 0, 0, 0);
      }
  }
  for (int kp = s_lo; kp < s_hi; ++kp) {
    bf16x8 bfr[4];
#pragma unroll
    for (int ct = 0; ct < 4; ++ct) bfr[ct] = *(const bf16x8*)(ub + 16 * (2 * ct + 4 * kp));
    const int e = 4 * (kp & 1) + g;
    const int R = 16 - c + (kp >> 1);
    const int pos = 8 * R + (e ^ (R & 7));
    const bf16x8 ah = *(const bf16x8*)(krh + 16 * pos);
    const bf16x8 al = *(const bf16x8*)(krl + 16 * pos);
#pragma unroll
    for (int ct = 0; ct < 4; ++ct) {
      acc[1][ct] = __builtin_amdgcn_mfma_f32_16x16x32_bf16(ah, bfr[ct], acc[1][ct], 0, 0, 0);
      acc[1][ct] = __builtin_amdgcn_mfma_f32_16x16x32_bf16(al, bfr[ct], acc[1][ct], 0, 0, 0);
    }
  }
  __syncthreads();
  STAMP(1, 2);
  float* yb_ = (float*)sm;
  if (w >= 4) {
#pragma unroll
    for (int tt = 0; tt < 2; ++tt)
#pragma unroll
      for (int ct = 0; ct < 4; ++ct) *(f32x4*)(yb_ + (((w - 4) * 8 + tt * 4 + ct) * 64 + lane) * 4) = acc[tt][ct];
  }
  __syncthreads();
  if (w < 4) {
#pragma unroll
    for (int tt = 0; tt < 2; ++tt)
#pragma unroll
      for (int ct = 0; ct < 4; ++ct) acc[tt][ct] += *(const f32x4*)(yb_ + ((w * 8 + tt * 4 + ct) * 64 + lane) * 4);
  }
  __syncthreads();
  if (w < 4) {
    float* yp = yb_ + w * 2048;
#pragma unroll
    for (int tt = 0; tt < 2; ++tt)
#pragma unroll
      for (int ct = 0; ct < 4; ++ct)
#pragma unroll
        for (int j = 0; j < 4; ++j) yp[64 * (16 * tt + 4 * g + j) + 16 * ct + c] = acc[tt][ct][j];
  }
  __syncthreads();
  const float* y0 = yb_ + 4 * t;
  f32x4 s0 = *(const f32x4*)(y0);
#pragma unroll
  for (int ww = 1; ww < 4; ++ww) s0 += *(const f32x4*)(y0 + ww * 2048);
  float r[4];
#pragma unroll
  for (int i = 0; i < 4; ++i) {
    const float xn = (i & 1) ? bfhi(xn_own[i >> 1]) : bflo(xn_own[i >> 1]);
    const float hh = rbf(rbf(xn * ca1) + sa);
    const float yb = rbf(s0[i] + k0 * hh);
    const float sk = rbf(hh * dsk);
    const float sy = rbf(yb + sk);
    r[i] = ga * sy;
  }
  ((u32x2*)(gyT + (size_t)d * L_))[t] = u32x2{pack2(r[0], r[1]), pack2(r[2], r[3])};
  STAMP(1, 3);
}

__global__ __launch_bounds__(320) void k3_ln2(const u16* __restrict__ gyT, const u16* __restrict__ x,
                                              const float* __restrict__ modv, u16* __restrict__ x1,
                                              char* __restrict__ h2F) {
  __shared__ float red[16 * 81];
  __shared__ float stat[32];
  const int t = threadIdx.x;
  const int l0 = ((blockIdx.x & 7) * 16 + (blockIdx.x >> 3)) * 16;
  STAMP(2, 0);
  const int cg = t & 3, ch = t >> 2;
  u32x2 v[8];
#pragma unroll
  for (int i = 0; i < 8; ++i) v[i] = *(const u32x2*)(gyT + (size_t)(8 * ch + i) * L_ + l0 + 4 * cg);
  u32x4 xr[4];
#pragma unroll
  for (int c = 0; c < 4; ++c) xr[c] = *(const u32x4*)(x + (size_t)(l0 + 4 * cg + c) * D_ + 8 * ch);
  const f32x4 sb0 = *(const f32x4*)(modv + 3 * 640 + 8 * ch), sb1 = *(const f32x4*)(modv + 3 * 640 + 8 * ch + 4);
  const f32x4 cb0 = *(const f32x4*)(modv + 4 * 640 + 8 * ch), cb1 = *(const f32x4*)(modv + 4 * 640 + 8 * ch + 4);
  u32x4 o[4];
#pragma unroll
  for (int dd = 0; dd < 2; ++dd)
#pragma unroll
    for (int p = 0; p < 4; ++p) {
      unsigned a = v[2 * p][dd], b = v[2 * p + 1][dd];
      o[2 * dd][p] = pack2(bflo(xr[2 * dd][p]) + bflo(a), bfhi(xr[2 * dd][p]) + bflo(b));
      o[2 * dd + 1][p] = pack2(bflo(xr[2 * dd + 1][p]) + bfhi(a), bfhi(xr[2 * dd + 1][p]) + bfhi(b));
    }
#pragma unroll
  for (int c = 0; c < 4; ++c) {
    float s = 0.f;
#pragma unroll
    for (int e = 0; e < 4; ++e) s += bflo(o[c][e]) + bfhi(o[c][e]);
    red[(4 * cg + c) * 81 + ch] = s;
  }
  __syncthreads();
  if (t < 256) {
    const int row = t >> 4, part = t & 15;
    float s = 0.f;
#pragma unroll
    for (int e = 0; e < 5; ++e) s += red[row * 81 + part * 5 + e];
#pragma unroll
    for (int of = 1; of < 16; of <<= 1) s += __shfl_xor(s, of);
    if (part == 0) stat[row] = s * (1.f / 640.f);
  }
  __syncthreads();
  STAMP(2, 1);
  float mean[4];
#pragma unroll
  for (int c = 0; c < 4; ++c) {
    mean[c] = stat[4 * cg + c];
    float s = 0.f;
#pragma unroll
    for (int e = 0; e < 4; ++e) {
      float a = bflo(o[c][e]) - mean[c], b = bfhi(o[c][e]) - mean[c];
      s += a * a + b * b;
    }
    red[(4 * cg + c) * 81 + ch] = s;
  }
  __syncthreads();
  if (t < 256) {
    const int row = t >> 4, part = t & 15;
    float s = 0.f;
#pragma unroll
    for (int e = 0; e < 5; ++e) s += red[row * 81 + part * 5 + e];
#pragma unroll
    for (int of = 1; of < 16; of <<= 1) s += __shfl_xor(s, of);
    if (part == 0) stat[16 + row] = s * (1.f / 640.f);
  }
  __syncthreads();
  STAMP(2, 2);
  float sbv[8] = {sb0[0], sb0[1], sb0[2], sb0[3], sb1[0], sb1[1], sb1[2], sb1[3]};
  float cbv[8] = {cb0[0], cb0[1], cb0[2], cb0[3], cb1[0], cb1[1], cb1[2], cb1[3]};
#pragma unroll
  for (int i = 0; i < 8; ++i) cbv[i] = rbf(1.f + cbv[i]);
  const int kt = ch >> 2, gq = ch & 3;
#pragma unroll
  for (int c = 0; c < 4; ++c) {
    const int m = l0 + 4 * cg + c;
    const float mb = rbf(mean[c]);
    const float den = rbf(sqrtf(rbf(rbf(stat[16 + 4 * cg + c]) + 1e-5f)));
    const float rden = frcp(den);
    u32x4 hv;
#pragma unroll
    for (int e = 0; e < 4; ++e) {
      float a = rbf(fdiv(rbf(bflo(o[c][e]) - mb), den, rden)), b = rbf(fdiv(rbf(bfhi(o[c][e]) - mb), den, rden));
      a = rbf(a * cbv[2 * e]) + sbv[2 * e];
      b = rbf(b * cbv[2 * e + 1]) + sbv[2 * e + 1];
      hv[e] = pack2(a, b);
    }
    NT_STORE((u32x4*)(h2F + ((size_t)((m >> 4) * 20 + kt)) * 1024 + (size_t)((m & 15) + 16 * gq) * 16), hv);
    NT_STORE((u32x4*)(x1 + (size_t)m * D_ + 8 * ch), o[c]);
  }
  STAMP(2, 3);
}

constexpr int G1_SLOT = 8192, G1_PF = 4;

__global__ __launch_bounds__(512) void k4_gemm1(const char* __restrict__ h2F, const char* __restrict__ W13F,
                                                char* __restrict__ hidF) {
  __shared__ __attribute__((aligned(16))) char sm[3 * G1_SLOT];
  const int t = threadIdx.x, w = t >> 6, lane = t & 63;
  const int xcd = blockIdx.x & 7, idx = blockIdx.x >> 3;
  const int nq = xcd >> 1, nstart = 7 * nq, ncnt = (nq == 3) ? 6 : 7;
  if (idx >= 4 * ncnt) return;
  const int bm = 4 * (xcd & 1) + (idx & 3), bn = nstart + (idx >> 2);
  STAMP(3, 0);
  const char* hsrc = h2F + ((size_t)((16 * bm + 2 * w) * 20)) * 1024 + lane * 16;
  const char* wsrc = W13F + ((size_t)((4 * bn + (w >> 1)) * 20)) * 2048 + (w & 1) * 1024 + lane * 16;
  f32x4 acc[4][2][2];
#pragma unroll
  for (int a = 0; a < 4; ++a)
#pragma unroll
    for (int b = 0; b < 2; ++b)
#pragma unroll
      for (int cI = 0; cI < 2; ++cI) acc[a][b][cI] = f32x4{0, 0, 0, 0};
  bf16x8 hf[G1_PF][2];
  u32x4 pw[G1_PF];
#define G1_LOADH(R, S) do { hf[R][0] = *(const bf16x8*)(hsrc + (size_t)(S) * 1024); \
    hf[R][1] = *(const bf16x8*)(hsrc + (size_t)(S) * 1024 + 20 * 1024); } while (0)
#define G1_LOADW(R, S) do { pw[R] = *(const u32x4*)(wsrc + (size_t)(S) * 2048); } while (0)
#define G1_WRITE(R, S) do { *(u32x4*)(sm + ((S) % 3) * G1_SLOT + w * 1024 + lane * 16) = pw[R]; } while (0)
#define G1_READ(B, S) do { const char* b_ = sm + ((S) % 3) * G1_SLOT; \
    _Pragma("unroll") for (int a = 0; a < 4; ++a) _Pragma("unroll") for (int b = 0; b < 2; ++b) \
      wa[B][a][b] = *(const bf16x8*)(b_ + (a * 2 + b) * 1024 + lane * 16); } while (0)
  bf16x8 wa[2][4][2];
#pragma unroll
  for (int s = 0; s < G1_PF; ++s) { G1_LOADW(s, s); G1_LOADH(s, s); }
  G1_WRITE(0, 0); G1_LOADW(0, G1_PF);
  G1_WRITE(1, 1); G1_LOADW(1, G1_PF + 1);
  __syncthreads();
  G1_READ(0, 0);
#pragma unroll
  for (int s = 0; s < 20; ++s) {
    if (s + 2 < 20) {
      G1_WRITE((s + 2) % G1_PF, s + 2);
      if (s + 2 + G1_PF < 20) G1_LOADW((s + 2) % G1_PF, s + 2 + G1_PF);
    }
    if (s + 1 < 20) G1_READ((s + 1) & 1, s + 1);
    __builtin_amdgcn_sched_barrier(0);
#pragma unroll
    for (int a = 0; a < 4; ++a)
#pragma unroll
      for (int b = 0; b < 2; ++b)
#pragma unroll
        for (int i = 0; i < 2; ++i)
          acc[a][b][i] = __builtin_amdgcn_mfma_f32_16x16x32_bf16(wa[s & 1][a][b], hf[s % G1_PF][i], acc[a][b][i], 0, 0, 0);
    if (s + G1_PF < 20) G1_LOADH(s % G1_PF, s + G1_PF);
    __builtin_amdgcn_sched_barrier(0);
    if (s + 2 < 20) __syncthreads();
  }
  STAMP(3, 1);
  const int cl = lane & 15, g = lane >> 4;
#pragma unroll
  for (int a = 0; a < 4; ++a) {
    const int nt = 4 * bn + a;
    const int kt2 = nt >> 1;
    const int slot = 16 * (2 * (nt & 1) + (g >> 1)) + cl;
#pragma unroll
    for (int i = 0; i < 2; ++i) {
      const int mt = 16 * bm + 2 * w + i;
      float hv[4];
#pragma unroll
      for (int j = 0; j < 4; ++j) {
        const float gv = rbf(acc[a][0][i][j]), uv = rbf(acc[a][1][i][j]);
        const float dn = 1.f + __expf(-gv);
        const float sv = rbf(fdiv(gv, dn, frcp(dn)));
        hv[j] = sv * uv;
      }
      u32x2 pv = {pack2(hv[0], hv[1]), pack2(hv[2], hv[3])};
      NT_STORE((u32x2*)(hidF + ((size_t)(mt * 54 + kt2)) * 1024 + slot * 16 + (g & 1) * 8), pv);
    }
  }
  STAMP(3, 2);
}

__global__ __launch_bounds__(512) void k5_gemm2(const char* __restrict__ hidF, const char* __restrict__ W2F,
                                                const u16* __restrict__ x1, const float* __restrict__ modv,
                                                u16* __restrict__ out) {
  __shared__ __attribute__((aligned(16))) float sm[8 * 20 * 256];
  const int t = threadIdx.x, w = t >> 6, lane = t & 63;
  const int bn = (blockIdx.x >> 3) & 7, bm = 4 * (blockIdx.x & 7) + (blockIdx.x >> 6);
  const int k_lo = (w < 6) ? 7 * w : 42 + 6 * (w - 6);
  const int nk = (w < 6) ? 7 : 6;
  f32x4 acc[5][4];
#pragma unroll
  for (int a = 0; a < 5; ++a)
#pragma unroll
    for (int b = 0; b < 4; ++b) acc[a][b] = f32x4{0, 0, 0, 0};
  STAMP(4, 0);
  const char* hp = hidF + ((size_t)(4 * bm) * 54 + k_lo) * 1024 + lane * 16;
  const char* wp = W2F + ((size_t)(5 * bn) * 54 + k_lo) * 1024 + lane * 16;
  bf16x8 hb[3][4], wa[3][5];
#pragma unroll
  for (int p = 0; p < 2; ++p) {
#pragma unroll
    for (int i = 0; i < 4; ++i) hb[p][i] = *(const bf16x8*)(hp + (size_t)(i * 54 + p) * 1024);
#pragma unroll
    for (int i = 0; i < 5; ++i) wa[p][i] = *(const bf16x8*)(wp + (size_t)(i * 54 + p) * 1024);
  }
  const int c = lane & 15, g = lane >> 4;
  const int t_lo = (w < 4) ? 3 * w : 12 + 2 * (w - 4);
  const int t_n = (w < 4) ? 3 : 2;
  u32x2 xv[3];
  f32x4 gbv[3];
#pragma unroll
  for (int q = 0; q < 3; ++q) {
    const int tile = (q < t_n) ? t_lo + q : t_lo;
    const int a = tile >> 2, b = tile & 3;
    const int m = 16 * (4 * bm + b) + c;
    const int n = 16 * (5 * bn + a) + 4 * g;
    xv[q] = *(const u32x2*)(x1 + (size_t)m * D_ + n);
    gbv[q] = *(const f32x4*)(modv + 5 * 640 + n);
  }
#pragma unroll
  for (int k = 0; k < 7; ++k) {
    if (k < nk) {
      if (k + 2 < nk) {
#pragma unroll
        for (int i = 0; i < 4; ++i) hb[(k + 2) % 3][i] = *(const bf16x8*)(hp + (size_t)(i * 54 + k + 2) * 1024);
#pragma unroll
        for (int i = 0; i < 5; ++i) wa[(k + 2) % 3][i] = *(const bf16x8*)(wp + (size_t)(i * 54 + k + 2) * 1024);
      }
#pragma unroll
      for (int a = 0; a < 5; ++a)
#pragma unroll
        for (int b = 0; b < 4; ++b)
          acc[a][b] = __builtin_amdgcn_mfma_f32_16x16x32_bf16(wa[k % 3][a], hb[k % 3][b], acc[a][b], 0, 0, 0);
    }
  }
  STAMP(4, 1);
#pragma unroll
  for (int a = 0; a < 5; ++a)
#pragma unroll
    for (int b = 0; b < 4; ++b) *(f32x4*)(sm + ((w * 20 + a * 4 + b) * 64 + lane) * 4) = acc[a][b];
  __syncthreads();
  STAMP(4, 2);
#pragma unroll
  for (int q = 0; q < 3; ++q) {
    if (q < t_n) {
      const int tile = t_lo + q;
      const int a = tile >> 2, b = tile & 3;
      const int m = 16 * (4 * bm + b) + c;
      const int n = 16 * (5 * bn + a) + 4 * g;
      f32x4 s = *(const f32x4*)(sm + ((0 * 20 + tile) * 64 + lane) * 4);
#pragma unroll
      for (int ww = 1; ww < 8; ++ww) s += *(const f32x4*)(sm + ((ww * 20 + tile) * 64 + lane) * 4);
      float r0 = bflo(xv[q][0]) + rbf(gbv[q][0] * rbf(s[0]));
      float r1 = bfhi(xv[q][0]) + rbf(gbv[q][1] * rbf(s[1]));
      float r2 = bflo(xv[q][1]) + rbf(gbv[q][2] * rbf(s[2]));
      float r3 = bfhi(xv[q][1]) + rbf(gbv[q][3] * rbf(s[3]));
      u32x2 ov = {pack2(r0, r1), pack2(r2, r3)};
      *(u32x2*)(out + (size_t)m * D_ + n) = ov;
    }
  }
  STAMP(4, 3);
}

extern "C" void kernel_launch(void* const* d_in, const int* in_sizes, int n_in, void* d_out, int out_size,
                              void* d_ws, size_t ws_size, hipStream_t stream) {
  const u16* x = (const u16*)d_in[0];
  const u16* A_re = (const u16*)d_in[1];
  const u16* A_im = (const u16*)d_in[2];
  const u16* B_re = (const u16*)d_in[3];
  const u16* B_im = (const u16*)d_in[4];
  const u16* C_re = (const u16*)d_in[5];
  const u16* C_im = (const u16*)d_in[6];
  const u16* log_dt = (const u16*)d_in[7];
  const u16* Dskip = (const u16*)d_in[8];
  const u16* w1 = (const u16*)d_in[10];
  const u16* w3 = (const u16*)d_in[11];
  const u16* w2 = (const u16*)d_in[12];
  const u16* cvec = (const u16*)d_in[13];
  const u16* w_mod = (const u16*)d_in[14];
  char* ws = (char*)d_ws;
  u16* out = (u16*)d_out;

  k1_prep<<<K1_KGEN + K1_LN + K1_MOD + K1_RT, 512, 0, stream>>>(x, A_re, A_im, B_re, B_im, C_re, C_im, log_dt,
                                                                 w1, w3, w2, cvec, w_mod, ws);
  k2_conv<<<640, 512, 0, stream>>>((const u16*)(ws + OFF_XNT), (const float*)(ws + OFF_KD),
                                   (const float*)(ws + OFF_MOD), Dskip, (u16*)(ws + OFF_GYT));
  k3_ln2<<<128, 320, 0, stream>>>((const u16*)(ws + OFF_GYT), x, (const float*)(ws + OFF_MOD), (u16*)(ws + OFF_X1),
                                  ws + OFF_H2F);
  k4_gemm1<<<8 * 28, 512, 0, stream>>>(ws + OFF_H2F, ws + OFF_W13F, ws + OFF_HIDF);
  k5_gemm2<<<32 * 8, 512, 0, stream>>>(ws + OFF_HIDF, ws + OFF_W2F, (const u16*)(ws + OFF_X1),
                                       (const float*)(ws + OFF_MOD), out);
}
```

```cpp
#include <hip/hip_runtime.h>
#include <hip/hip_bf16.h>

typedef __attribute__((ext_vector_type(8))) short bf16x8;
typedef __attribute__((ext_vector_type(4))) float f32x4;
typedef __attribute__((ext_vector_type(4))) unsigned u32x4;
typedef __attribute__((ext_vector_type(2))) unsigned u32x2;
typedef unsigned short u16;

#define DI __device__ __forceinline__
#define NT_STORE(P, V) (*(P) = (V))
#define STAMP(K, I)

constexpr int L_ = 2048, D_ = 640, N_ = 128, F_ = 1728;

constexpr size_t OFF_MOD = 0;
constexpr size_t OFF_K0 = 16384;
constexpr size_t OFF_XNT = 32768;
constexpr size_t SZ_ACT = (size_t)D_ * L_ * 2;
constexpr int KD_STRIDE = 648;
constexpr size_t OFF_KD = OFF_XNT + SZ_ACT;
constexpr size_t OFF_GYT = OFF_KD + (size_t)D_ * KD_STRIDE * 4;
constexpr size_t OFF_X1 = OFF_GYT + SZ_ACT;
constexpr size_t OFF_H2F = OFF_X1 + SZ_ACT;
constexpr size_t OFF_W13F = OFF_H2F + SZ_ACT;
constexpr size_t SZ_W = (size_t)D_ * F_ * 2;
constexpr size_t OFF_W2F = OFF_W13F + 2 * SZ_W;
constexpr size_t OFF_HIDF = OFF_W2F + SZ_W;

DI float bf2f(unsigned v) { return __uint_as_float(v << 16); }
DI float bflo(unsigned v) { return __uint_as_float(v << 16); }
DI float bfhi(unsigned v) { return __uint_as_float(v & 0xffff0000u); }
DI float rbf(float x) { return (float)(__bf16)x; }
DI unsigned f2bf(float x) { __bf16 b = (__bf16)x; return (unsigned)__builtin_bit_cast(u16, b); }
DI unsigned pack2(float lo, float hi) { return f2bf(lo) | (f2bf(hi) << 16); }
DI float frcp(float x) { return __builtin_amdgcn_rcpf(x); }
DI float fdiv(float a, float b, float rb) { float q = a * rb; float r = fmaf(-q, b, a); return fmaf(r, rb, q); }

DI void transpose8x8(const u32x4* in, u32x4* out) {
#pragma unroll
  for (int d = 0; d < 4; ++d) {
#pragma unroll
    for (int p = 0; p < 4; ++p) {
      unsigned a = in[2 * p][d], b = in[2 * p + 1][d];
      out[2 * d][p] = (a & 0xffffu) | (b << 16);
      out[2 * d + 1][p] = (a >> 16) | (b & 0xffff0000u);
    }
  }
}

typedef __attribute__((ext_vector_type(2))) float c32;
DI c32 cexp_l(float ar, float ai, float l) {
  const float mag = __expf(ar * l);
  float rev = (ai * l) * 0.15915494309189535f;
  rev = rev - floorf(rev);
  c32 cs = {__builtin_amdgcn_cosf(rev), __builtin_amdgcn_sinf(rev)};
  return cs * mag;
}
DI c32 cmul(c32 a, c32 b) {
  c32 t, r;
  asm("v_pk_mul_f32 %0, %1, %2 op_sel_hi:[0,1]" : "=v"(t) : "v"(a), "v"(b));
  asm("v_pk_fma_f32 %0, %1, %2, %3 op_sel:[1,1,0] op_sel_hi:[1,0,1] neg_lo:[0,1,0]\n\ts_nop 1"
      : "=v"(r) : "v"(a), "v"(b), "v"(t));
  return r;
}

DI void retile_task(int task, const u16* __restrict__ w1, const u16* __restrict__ w3, const u16* __restrict__ w2,
                    char* __restrict__ ws) {
  if (task >= 3 * 17280) return;
  const int which = task / 17280, tk = task % 17280;
  u32x4 v[8], o[8];
  if (which < 2) {
    const int nc = tk % 216, kc = tk / 216;
    const u16* src = which == 0 ? w1 : w3;
#pragma unroll
    for (int i = 0; i < 8; ++i) v[i] = *(const u32x4*)(src + (size_t)(8 * kc + i) * F_ + 8 * nc);
    transpose8x8(v, o);
    const int nt = nc >> 1, kt = kc >> 2, g = kc & 3;
    char* dst = ws + OFF_W13F + ((size_t)((nt * 20 + kt) * 2 + which)) * 1024 + (size_t)(16 * g + 8 * (nc & 1)) * 16;
#pragma unroll
    for (int c = 0; c < 8; ++c) *(u32x4*)(dst + c * 16) = o[c];
  } else {
    const int nc = tk % 80, kc = tk / 80;
#pragma unroll
    for (int i = 0; i < 8; ++i) v[i] = *(const u32x4*)(w2 + (size_t)(8 * kc + i) * D_ + 8 * nc);
    transpose8x8(v, o);
    const int nt = nc >> 1, kt = kc >> 2, g = kc & 3;
    char* dst = ws + OFF_W2F + ((size_t)(nt * 54 + kt)) * 1024 + (size_t)(16 * g + 8 * (nc & 1)) * 16;
#pragma unroll
    for (int c = 0; c < 8; ++c) *(u32x4*)(dst + c * 16) = o[c];
  }
}

constexpr int K1_KGEN = 640, K1_LN = 256, K1_MOD = 120, K1_RT = 0, K2_RT = 102;

__global__ __launch_bounds__(512) void k1_prep(
    const u16* __restrict__ x, const u16* __restrict__ A_re, const u16* __restrict__ A_im,
    const u16* __restrict__ B_re, const u16* __restrict__ B_im, const u16* __restrict__ C_re,
    const u16* __restrict__ C_im, const u16* __restrict__ log_dt, const u16* __restrict__ cvec,
    const u16* __restrict__ w_mod, char* __restrict__ ws) {
  __shared__ __attribute__((aligned(16))) float sm[8 * 4 * 256 + 1536];
  const int t = threadIdx.x;
  int bid = blockIdx.x;
  STAMP(0, 0);
  if (bid < K1_KGEN) {
    const int d = bid;
    const int w = t >> 6, lane = t & 63, i = lane & 15, kk = lane >> 4;
    float* prm = sm + 8192;
    if (t < 128) {
      const int n = d * N_ + t;
      const float dt = rbf(__expf(bf2f(log_dt[d])));
      const float ar = dt * bf2f(A_re[n]), ai = dt * bf2f(A_im[n]);
      const c32 db = {dt * bf2f(B_re[n]), dt * bf2f(B_im[n])};
      const c32 cc = {bf2f(C_re[n]), bf2f(C_im[n])};
      const c32 c0 = cmul(cc, db);
      c32 gg = cexp_l(ar, ai, 256.f);
      const c32 c1 = cmul(gg, c0);
      gg = cmul(gg, gg);
      const c32 c2 = cmul(gg, c0);
      gg = cmul(gg, gg);
      const c32 c3 = cmul(gg, c0);
      *(f32x4*)(prm + t * 12) = f32x4{ar, ai, c0.x, c0.y};
      *(f32x4*)(prm + t * 12 + 4) = f32x4{c1.x, c1.y, c2.x, c2.y};
      *(f32x4*)(prm + t * 12 + 8) = f32x4{c3.x, c3.y, 0.f, 0.f};
    }
    __syncthreads();
    STAMP(0, 3);
    f32x4 acc0 = {0, 0, 0, 0}, acc1 = {0, 0, 0, 0}, acc2 = {0, 0, 0, 0}, acc3 = {0, 0, 0, 0};
    const float fi = (float)i;
#pragma unroll
    for (int s = 0; s < 4; ++s) {
      const int n = 16 * w + 4 * s + kk;
      const f32x4 p0 = *(const f32x4*)(prm + n * 12), p1 = *(const f32x4*)(prm + n * 12 + 4), p2 = *(const f32x4*)(prm + n * 12 + 8);
      c32 e = cexp_l(p0[0], p0[1], fi);
      c32 xx = cexp_l(p0[0], p0[1], 16.f * fi);
      c32 pp;
      pp = cmul(xx, c32{p0[2], p0[3]});
      acc0 = __builtin_amdgcn_mfma_f32_16x16x4f32(pp.x, e.x, acc0, 0, 0, 0);
      acc0 = __builtin_amdgcn_mfma_f32_16x16x4f32(-pp.y, e.y, acc0, 0, 0, 0);
      e = cmul(e, e); xx = cmul(xx, xx);
      pp = cmul(xx, c32{p1[0], p1[1]});
      acc1 = __builtin_amdgcn_mfma_f32_16x16x4f32(pp.x, e.x, acc1, 0, 0, 0);
      acc1 = __builtin_amdgcn_mfma_f32_16x16x4f32(-pp.y, e.y, acc1, 0, 0, 0);
      e = cmul(e, e); xx = cmul(xx, xx);
      pp = cmul(xx, c32{p1[2], p1[3]});
      acc2 = __builtin_amdgcn_mfma_f32_16x16x4f32(pp.x, e.x, acc2, 0, 0, 0);
      acc2 = __builtin_amdgcn_mfma_f32_16x16x4f32(-pp.y, e.y, acc2, 0, 0, 0);
      e = cmul(e, e); xx = cmul(xx, xx);
      pp = cmul(xx, c32{p2[0], p2[1]});
      acc3 = __builtin_amdgcn_mfma_f32_16x16x4f32(pp.x, e.x, acc3, 0, 0, 0);
      acc3 = __builtin_amdgcn_mfma_f32_16x16x4f32(-pp.y, e.y, acc3, 0, 0, 0);
    }
    STAMP(0, 1);
    float* red = sm;
#pragma unroll
    for (int j = 0; j < 4; ++j) {
      const int idx = 16 * (4 * kk + j) + i;
      red[(w * 4 + 0) * 256 + idx] = acc0[j];
      red[(w * 4 + 1) * 256 + idx] = acc1[j];
      red[(w * 4 + 2) * 256 + idx] = acc2[j];
      red[(w * 4 + 3) * 256 + idx] = acc3[j];
    }
    __syncthreads();
    STAMP(0, 4);
    float* kd = (float*)(ws + OFF_KD) + (size_t)d * KD_STRIDE;
    for (int ci = t; ci < 641; ci += 512) {
      int tile, idx;
      if (ci < 256) { tile = 0; idx = ci; }
      else if (ci < 384) { tile = 1; idx = ci - 256; }
      else if (ci < 512) { tile = 2; idx = ci - 384; }
      else { tile = 3; idx = ci - 512; }
      float v = 0.f;
#pragma unroll
      for (int ww = 0; ww < 8; ++ww) v += red[(ww * 4 + tile) * 256 + idx];
      kd[ci] = v;
    }
    STAMP(0, 2);
    return;
  }
  bid -= K1_KGEN;
  if (bid < K1_LN) {
    const int l0 = ((bid & 7) * 32 + (bid >> 3)) * 8;
    const int w = t >> 6, lane = t & 63;
    const unsigned* xr = (const unsigned*)(x + (size_t)(l0 + w) * D_);
    unsigned v[5];
#pragma unroll
    for (int j = 0; j < 5; ++j) v[j] = xr[lane + 64 * j];
    float s = 0.f;
#pragma unroll
    for (int j = 0; j < 5; ++j) s += bflo(v[j]) + bfhi(v[j]);
    STAMP(0, 3);
#pragma unroll
    for (int o = 1; o < 64; o <<= 1) s += __shfl_xor(s, o);
    const float mean = s * (1.f / 640.f);
    float q = 0.f;
#pragma unroll
    for (int j = 0; j < 5; ++j) {
      float a = bflo(v[j]) - mean, b = bfhi(v[j]) - mean;
      q += a * a + b * b;
    }
#pragma unroll
    for (int o = 1; o < 64; o <<= 1) q += __shfl_xor(q, o);
    const float mb = rbf(mean);
    const float den = rbf(sqrtf(rbf(rbf(q * (1.f / 640.f)) + 1e-5f)));
    const float rden = frcp(den);
    unsigned* sn = (unsigned*)sm;
#pragma unroll
    for (int j = 0; j < 5; ++j) {
      float a = fdiv(rbf(bflo(v[j]) - mb), den, rden), b = fdiv(rbf(bfhi(v[j]) - mb), den, rden);
      sn[w * 320 + lane + 64 * j] = pack2(a, b);
    }
    __syncthreads();
    STAMP(0, 4);
    const u16* sn16 = (const u16*)sn;
    u16* xnT = (u16*)(ws + OFF_XNT);
    for (int dd = t; dd < 640; dd += 512) {
      u32x4 a;
#pragma unroll
      for (int e = 0; e < 4; ++e)
        a[e] = (unsigned)sn16[(2 * e) * 640 + dd] | ((unsigned)sn16[(2 * e + 1) * 640 + dd] << 16);
      *(u32x4*)(xnT + (size_t)dd * L_ + l0) = a;
    }
    STAMP(0, 2);
    return;
  }
  bid -= K1_LN;
  if (bid < K1_MOD) {
    const int j0 = bid * 32;
    const int cc = t & 3, rgp = t >> 2;
    u32x4 wv[5];
    u16 cv[5];
#pragma unroll
    for (int r = 0; r < 5; ++r) {
      const int i = rgp * 5 + r;
      wv[r] = *(const u32x4*)(w_mod + (size_t)i * 3840 + j0 + 8 * cc);
      cv[r] = cvec[i];
    }
    STAMP(0, 3);
    float acc[8];
#pragma unroll
    for (int e = 0; e < 8; ++e) acc[e] = 0.f;
#pragma unroll
    for (int r = 0; r < 5; ++r) {
      const float c_ = bf2f(cv[r]);
      const float sv = rbf(c_ / (1.f + __expf(-c_)));
#pragma unroll
      for (int e = 0; e < 4; ++e) {
        acc[2 * e] += sv * bflo(wv[r][e]);
        acc[2 * e + 1] += sv * bfhi(wv[r][e]);
      }
    }
    float* red = sm;
#pragma unroll
    for (int e = 0; e < 8; ++e) red[rgp * 33 + 8 * cc + e] = acc[e];
    __syncthreads();
    {
      const int col = t >> 4, part = t & 15;
      float s = 0.f;
#pragma unroll
      for (int r = 0; r < 8; ++r) s += red[(part * 8 + r) * 33 + col];
#pragma unroll
      for (int o = 1; o < 16; o <<= 1) s += __shfl_xor(s, o);
      if (part == 0) ((float*)(ws + OFF_MOD))[j0 + col] = rbf(s);
    }
    STAMP(0, 2);
    return;
  }
}

constexpr int UC_STRIDE = 4608;
constexpr int UC_BYTES = 8 * UC_STRIDE;
constexpr int KR_BYTES = 384 * 16;
constexpr int K2_LDS = UC_BYTES + 2 * KR_BYTES;

DI int lag_to_ci(int lag) {
  const int r = (int)rbf((float)lag);
  if (r < 256) return r;
  if (r < 512) return 256 + ((r - 256) >> 1);
  if (r < 1024) return 384 + ((r - 512) >> 2);
  return 512 + ((r - 1024) >> 3);
}

__global__ __launch_bounds__(512) void k2_conv(const u16* __restrict__ xnT, const float* __restrict__ KD,
                                               const float* __restrict__ modv, const u16* __restrict__ Dskip,
                                               u16* __restrict__ gyT, const u16* __restrict__ w1,
                                               const u16* __restrict__ w3, const u16* __restrict__ w2,
                                               char* __restrict__ ws) {
  __shared__ __attribute__((aligned(16))) char sm[K2_LDS];
  if (blockIdx.x >= 640) { retile_task((blockIdx.x - 640) * 512 + threadIdx.x, w1, w3, w2, ws); return; }
  char* uc = sm;
  char* krh = sm + UC_BYTES;
  char* krl = krh + KR_BYTES;
  const int d = blockIdx.x, t = threadIdx.x;
  const int w = t >> 6, lane = t & 63, c = lane & 15, g = lane >> 4;
  STAMP(1, 0);
  const float* kd = KD + (size_t)d * KD_STRIDE;
  const float sa = modv[d], ca1 = rbf(1.f + modv[640 + d]), ga = modv[1280 + d];
  const float dsk = bf2f(Dskip[d]), k0 = kd[0];
  const u32x2 xn_own = ((const u32x2*)(xnT + (size_t)d * L_))[t];

  if (t < 256) {
    const u32x4* xn4 = (const u32x4*)(xnT + (size_t)d * L_);
    u32x4 cur = xn4[t];
    u32x4 prv = {0, 0, 0, 0};
    if (t > 0) prv = xn4[t - 1];
    unsigned hv[8];
#pragma unroll
    for (int e = 0; e < 4; ++e) {
      float a = rbf(rbf(bflo(prv[e]) * ca1) + sa), b = rbf(rbf(bfhi(prv[e]) * ca1) + sa);
      hv[e] = (t > 0) ? pack2(a, b) : 0u;
      float a2 = rbf(rbf(bflo(cur[e]) * ca1) + sa), b2 = rbf(rbf(bfhi(cur[e]) * ca1) + sa);
      hv[4 + e] = pack2(a2, b2);
    }
#pragma unroll
    for (int s = 0; s < 8; ++s) {
      u32x4 o;
      if ((s & 1) == 0) {
#pragma unroll
        for (int e = 0; e < 4; ++e) o[e] = hv[s / 2 + e];
      } else {
#pragma unroll
        for (int e = 0; e < 4; ++e) o[e] = (hv[s / 2 + e] >> 16) | (hv[s / 2 + e + 1] << 16);
      }
      *(u32x4*)(uc + s * UC_STRIDE + 64 * (s & 3) + 32 * (s >> 2) + 16 * (t + 7)) = o;
    }
  } else {
    const int t2 = t - 256;
    float kv[8];
#pragma unroll
    for (int u = 0; u < 8; ++u) {
      const int lag = 2048 - (8 * t2 + u);
      kv[u] = kd[lag_to_ci(lag < 2048 ? lag : 2047)];
      if (lag == 2048) kv[u] = 0.f;
    }
    if (t2 < 56) {
      const int s = t2 / 7, m = t2 % 7;
      *(u32x4*)(uc + s * UC_STRIDE + 64 * (s & 3) + 32 * (s >> 2) + 16 * m) = u32x4{0, 0, 0, 0};
    }
    if (t2 < 120) {
      *(u32x4*)(krh + 16 * (264 + t2)) = u32x4{0, 0, 0, 0};
      *(u32x4*)(krl + 16 * (264 + t2)) = u32x4{0, 0, 0, 0};
    }
    u32x4 kh, kl;
#pragma unroll
    for (int e = 0; e < 4; ++e) {
      const float h0 = rbf(kv[2 * e]), h1 = rbf(kv[2 * e + 1]);
      kh[e] = pack2(h0, h1);
      kl[e] = pack2(kv[2 * e] - h0, kv[2 * e + 1] - h1);
    }
    const int ci = 8 + t2, R = ci >> 3, e = ci & 7;
    const int pos = 8 * R + (e ^ (R & 7));
    *(u32x4*)(krh + 16 * pos) = kh;
    *(u32x4*)(krl + 16 * pos) = kl;
  }
  __syncthreads();

  STAMP(1, 1);
  f32x4 acc[2][4];
#pragma unroll
  for (int a = 0; a < 2; ++a)
#pragma unroll
    for (int b = 0; b < 4; ++b) acc[a][b] = f32x4{0, 0, 0, 0};

  const int sg = c & 7;
  const char* ub = uc + sg * UC_STRIDE + 64 * (sg & 3) + 32 * (sg >> 2) + 16 * ((c >> 3) + g);
  int dA_lo, dA_hi, s_lo, s_hi;
  if (w < 5) { dA_lo = 6 * w; dA_hi = 6 * w + 6; s_lo = 32; s_hi = 32; }
  else if (w == 5) { dA_lo = 30; dA_hi = 32; s_lo = 32; s_hi = 40; }
  else { dA_lo = 32; dA_hi = 32; s_lo = 40 + 12 * (w - 6); s_hi = s_lo + 12; }
  for (int kp = dA_lo; kp < dA_hi; ++kp) {
    bf16x8 bfr[4];
#pragma unroll
    for (int ct = 0; ct < 4; ++ct) bfr[ct] = *(const bf16x8*)(ub + 16 * (2 * ct + 4 * kp));
    const int e = 4 * (kp & 1) + g;
    bf16x8 ah[2], al[2];
#pragma unroll
    for (int tt = 0; tt < 2; ++tt) {
      const int R = 32 - 16 * tt - c + (kp >> 1);
      const int pos = 8 * R + (e ^ (R & 7));
      ah[tt] = *(const bf16x8*)(krh + 16 * pos);
      al[tt] = *(const bf16x8*)(krl + 16 * pos);
    }
#pragma unroll
    for (int tt = 0; tt < 2; ++tt)
#pragma unroll
      for (int ct = 0; ct < 4; ++ct) {
        acc[tt][ct] = __builtin_amdgcn_mfma_f32_16x16x32_bf16(ah[tt], bfr[ct], acc[tt][ct], 0, 0, 0);
        acc[tt][ct] = __builtin_amdgcn_mfma_f32_16x16x32_bf16(al[tt], bfr[ct], acc[tt][ct], 0, 0, 0);
      }
  }
  for (int kp = s_lo; kp < s_hi; ++kp) {
    bf16x8 bfr[4];
#pragma unroll
    for (int ct = 0; ct < 4; ++ct) bfr[ct] = *(const bf16x8*)(ub + 16 * (2 * ct + 4 * kp));
    const int e = 4 * (kp & 1) + g;
    const int R = 16 - c + (kp >> 1);
    const int pos = 8 * R + (e ^ (R & 7));
    const bf16x8 ah = *(const bf16x8*)(krh + 16 * pos);
    const bf16x8 al = *(const bf16x8*)(krl + 16 * pos);
#pragma unroll
    for (int ct = 0; ct < 4; ++ct) {
      acc[1][ct] = __builtin_amdgcn_mfma_f32_16x16x32_bf16(ah, bfr[ct], acc[1][ct], 0, 0, 0);
      acc[1][ct] = __builtin_amdgcn_mfma_f32_16x16x32_bf16(al, bfr[ct], acc[1][ct], 0, 0, 0);
    }
  }
  __syncthreads();
  STAMP(1, 2);
  float* yb_ = (float*)sm;
  if (w >= 4) {
#pragma unroll
    for (int tt = 0; tt < 2; ++tt)
#pragma unroll
      for (int ct = 0; ct < 4; ++ct) *(f32x4*)(yb_ + (((w - 4) * 8 + tt * 4 + ct) * 64 + lane) * 4) = acc[tt][ct];
  }
  __syncthreads();
  if (w < 4) {
#pragma unroll
    for (int tt = 0; tt < 2; ++tt)
#pragma unroll
      for (int ct = 0; ct < 4; ++ct) acc[tt][ct] += *(const f32x4*)(yb_ + ((w * 8 + tt * 4 + ct) * 64 + lane) * 4);
  }
  __syncthreads();
  if (w < 4) {
    float* yp = yb_ + w * 2048;
#pragma unroll
    for (int tt = 0; tt < 2; ++tt)
#pragma unroll
      for (int ct = 0; ct < 4; ++ct)
#pragma unroll
        for (int j = 0; j < 4; ++j) yp[64 * (16 * tt + 4 * g + j) + 16 * ct + c] = acc[tt][ct][j];
  }
  __syncthreads();
  const float* y0 = yb_ + 4 * t;
  f32x4 s0 = *(const f32x4*)(y0);
#pragma unroll
  for (int ww = 1; ww < 4; ++ww) s0 += *(const f32x4*)(y0 + ww * 2048);
  float r[4];
#pragma unroll
  for (int i = 0; i < 4; ++i) {
    const float xn = (i & 1) ? bfhi(xn_own[i >> 1]) : bflo(xn_own[i >> 1]);
    const float hh = rbf(rbf(xn * ca1) + sa);
    const float yb = rbf(s0[i] + k0 * hh);
    const float sk = rbf(hh * dsk);
    const float sy = rbf(yb + sk);
    r[i] = ga * sy;
  }
  ((u32x2*)(gyT + (size_t)d * L_))[t] = u32x2{pack2(r[0], r[1]), pack2(r[2], r[3])};
  STAMP(1, 3);
}

__global__ __launch_bounds__(320) void k3_ln2(const u16* __restrict__ gyT, const u16* __restrict__ x,
                                              const float* __restrict__ modv, u16* __restrict__ x1,
                                              char* __restrict__ h2F) {
  __shared__ float red[16 * 81];
  __shared__ float stat[32];
  const int t = threadIdx.x;
  const int l0 = ((blockIdx.x & 7) * 16 + (blockIdx.x >> 3)) * 16;
  STAMP(2, 0);
  const int cg = t & 3, ch = t >> 2;
  u32x2 v[8];
#pragma unroll
  for (int i = 0; i < 8; ++i) v[i] = *(const u32x2*)(gyT + (size_t)(8 * ch + i) * L_ + l0 + 4 * cg);
  u32x4 xr[4];
#pragma unroll
  for (int c = 0; c < 4; ++c) xr[c] = *(const u32x4*)(x + (size_t)(l0 + 4 * cg + c) * D_ + 8 * ch);
  const f32x4 sb0 = *(const f32x4*)(modv + 3 * 640 + 8 * ch), sb1 = *(const f32x4*)(modv + 3 * 640 + 8 * ch + 4);
  const f32x4 cb0 = *(const f32x4*)(modv + 4 * 640 + 8 * ch), cb1 = *(const f32x4*)(modv + 4 * 640 + 8 * ch + 4);
  u32x4 o[4];
#pragma unroll
  for (int dd = 0; dd < 2; ++dd)
#pragma unroll
    for (int p = 0; p < 4; ++p) {
      unsigned a = v[2 * p][dd], b = v[2 * p + 1][dd];
      o[2 * dd][p] = pack2(bflo(xr[2 * dd][p]) + bflo(a), bfhi(xr[2 * dd][p]) + bflo(b));
      o[2 * dd + 1][p] = pack2(bflo(xr[2 * dd + 1][p]) + bfhi(a), bfhi(xr[2 * dd + 1][p]) + bfhi(b));
    }
#pragma unroll
  for (int c = 0; c < 4; ++c) {
    float s = 0.f;
#pragma unroll
    for (int e = 0; e < 4; ++e) s += bflo(o[c][e]) + bfhi(o[c][e]);
    red[(4 * cg + c) * 81 + ch] = s;
  }
  __syncthreads();
  if (t < 256) {
    const int row = t >> 4, part = t & 15;
    float s = 0.f;
#pragma unroll
    for (int e = 0; e < 5; ++e) s += red[row * 81 + part * 5 + e];
#pragma unroll
    for (int of = 1; of < 16; of <<= 1) s += __shfl_xor(s, of);
    if (part == 0) stat[row] = s * (1.f / 640.f);
  }
  __syncthreads();
  STAMP(2, 1);
  float mean[4];
#pragma unroll
  for (int c = 0; c < 4; ++c) {
    mean[c] = stat[4 * cg + c];
    float s = 0.f;
#pragma unroll
    for (int e = 0; e < 4; ++e) {
      float a = bflo(o[c][e]) - mean[c], b = bfhi(o[c][e]) - mean[c];
      s += a * a + b * b;
    }
    red[(4 * cg + c) * 81 + ch] = s;
  }
  __syncthreads();
  if (t < 256) {
    const int row = t >> 4, part = t & 15;
    float s = 0.f;
#pragma unroll
    for (int e = 0; e < 5; ++e) s += red[row * 81 + part * 5 + e];
#pragma unroll
    for (int of = 1; of < 16; of <<= 1) s += __shfl_xor(s, of);
    if (part == 0) stat[16 + row] = s * (1.f / 640.f);
  }
  __syncthreads();
  STAMP(2, 2);
  float sbv[8] = {sb0[0], sb0[1], sb0[2], sb0[3], sb1[0], sb1[1], sb1[2], sb1[3]};
  float cbv[8] = {cb0[0], cb0[1], cb0[2], cb0[3], cb1[0], cb1[1], cb1[2], cb1[3]};
#pragma unroll
  for (int i = 0; i < 8; ++i) cbv[i] = rbf(1.f + cbv[i]);
  const int kt = ch >> 2, gq = ch & 3;
#pragma unroll
  for (int c = 0; c < 4; ++c) {
    const int m = l0 + 4 * cg + c;
    const float mb = rbf(mean[c]);
    const float den = rbf(sqrtf(rbf(rbf(stat[16 + 4 * cg + c]) + 1e-5f)));
    const float rden = frcp(den);
    u32x4 hv;
#pragma unroll
    for (int e = 0; e < 4; ++e) {
      float a = rbf(fdiv(rbf(bflo(o[c][e]) - mb), den, rden)), b = rbf(fdiv(rbf(bfhi(o[c][e]) - mb), den, rden));
      a = rbf(a * cbv[2 * e]) + sbv[2 * e];
      b = rbf(b * cbv[2 * e + 1]) + sbv[2 * e + 1];
      hv[e] = pack2(a, b);
    }
    NT_STORE((u32x4*)(h2F + ((size_t)((m >> 4) * 20 + kt)) * 1024 + (size_t)((m & 15) + 16 * gq) * 16), hv);
    NT_STORE((u32x4*)(x1 + (size_t)m * D_ + 8 * ch), o[c]);
  }
  STAMP(2, 3);
}

constexpr int G1_SLOT = 8192, G1_PF = 4;

__global__ __launch_bounds__(512) void k4_gemm1(const char* __restrict__ h2F, const char* __restrict__ W13F,
                                                char* __restrict__ hidF) {
  __shared__ __attribute__((aligned(16))) char sm[3 * G1_SLOT];
  const int t = threadIdx.x, w = t >> 6, lane = t & 63;
  const int xcd = blockIdx.x & 7, idx = blockIdx.x >> 3;
  const int nq = xcd >> 1, nstart = 7 * nq, ncnt = (nq == 3) ? 6 : 7;
  if (idx >= 4 * ncnt) return;
  const int bm = 4 * (xcd & 1) + (idx & 3), bn = nstart + (idx >> 2);
  STAMP(3, 0);
  const char* hsrc = h2F + ((size_t)((16 * bm + 2 * w) * 20)) * 1024 + lane * 16;
  const char* wsrc = W13F + ((size_t)((4 * bn + (w >> 1)) * 20)) * 2048 + (w & 1) * 1024 + lane * 16;
  f32x4 acc[4][2][2];
#pragma unroll
  for (int a = 0; a < 4; ++a)
#pragma unroll
    for (int b = 0; b < 2; ++b)
#pragma unroll
      for (int cI = 0; cI < 2; ++cI) acc[a][b][cI] = f32x4{0, 0, 0, 0};
  bf16x8 hf[G1_PF][2];
  u32x4 pw[G1_PF];
#define G1_LOADH(R, S) do { hf[R][0] = *(const bf16x8*)(hsrc + (size_t)(S) * 1024); \
    hf[R][1] = *(const bf16x8*)(hsrc + (size_t)(S) * 1024 + 20 * 1024); } while (0)
#define G1_LOADW(R, S) do { pw[R] = *(const u32x4*)(wsrc + (size_t)(S) * 2048); } while (0)
#define G1_WRITE(R, S) do { *(u32x4*)(sm + ((S) % 3) * G1_SLOT + w * 1024 + lane * 16) = pw[R]; } while (0)
#define G1_READ(B, S) do { const char* b_ = sm + ((S) % 3) * G1_SLOT; \
    _Pragma("unroll") for (int a = 0; a < 4; ++a) _Pragma("unroll") for (int b = 0; b < 2; ++b) \
      wa[B][a][b] = *(const bf16x8*)(b_ + (a * 2 + b) * 1024 + lane * 16); } while (0)
  bf16x8 wa[2][4][2];
#pragma unroll
  for (int s = 0; s < G1_PF; ++s) { G1_LOADW(s, s); G1_LOADH(s, s); }
  G1_WRITE(0, 0); G1_LOADW(0, G1_PF);
  G1_WRITE(1, 1); G1_LOADW(1, G1_PF + 1);
  __syncthreads();
  G1_READ(0, 0);
#pragma unroll
  for (int s = 0; s < 20; ++s) {
    if (s + 2 < 20) {
      G1_WRITE((s + 2) % G1_PF, s + 2);
      if (s + 2 + G1_PF < 20) G1_LOADW((s + 2) % G1_PF, s + 2 + G1_PF);
    }
    if (s + 1 < 20) G1_READ((s + 1) & 1, s + 1);
    __builtin_amdgcn_sched_barrier(0);
#pragma unroll
    for (int a = 0; a < 4; ++a)
#pragma unroll
      for (int b = 0; b < 2; ++b)
#pragma unroll
        for (int i = 0; i < 2; ++i)
          acc[a][b][i] = __builtin_amdgcn_mfma_f32_16x16x32_bf16(wa[s & 1][a][b], hf[s % G1_PF][i], acc[a][b][i], 0, 0, 0);
    if (s + G1_PF < 20) G1_LOADH(s % G1_PF, s + G1_PF);
    __builtin_amdgcn_sched_barrier(0);
    if (s + 2 < 20) __syncthreads();
  }
  STAMP(3, 1);
  const int cl = lane & 15, g = lane >> 4;
#pragma unroll
  for (int a = 0; a < 4; ++a) {
    const int nt = 4 * bn + a;
    const int kt2 = nt >> 1;
    const int slot = 16 * (2 * (nt & 1) + (g >> 1)) + cl;
#pragma unroll
    for (int i = 0; i < 2; ++i) {
      const int mt = 16 * bm + 2 * w + i;
      float hv[4];
#pragma unroll
      for (int j = 0; j < 4; ++j) {
        const float gv = rbf(acc[a][0][i][j]), uv = rbf(acc[a][1][i][j]);
        const float dn = 1.f + __expf(-gv);
        const float sv = rbf(fdiv(gv, dn, frcp(dn)));
        hv[j] = sv * uv;
      }
      u32x2 pv = {pack2(hv[0], hv[1]), pack2(hv[2], hv[3])};
      NT_STORE((u32x2*)(hidF + ((size_t)(mt * 54 + kt2)) * 1024 + slot * 16 + (g & 1) * 8), pv);
    }
  }
  STAMP(3, 2);
}

__global__ __launch_bounds__(512) void k5_gemm2(const char* __restrict__ hidF, const char* __restrict__ W2F,
                                                const u16* __restrict__ x1, const float* __restrict__ modv,
                                                u16* __restrict__ out) {
  __shared__ __attribute__((aligned(16))) float sm[8 * 20 * 256];
  const int t = threadIdx.x, w = t >> 6, lane = t & 63;
  const int bn = (blockIdx.x >> 3) & 7, bm = 4 * (blockIdx.x & 7) + (blockIdx.x >> 6);
  const int k_lo = (w < 6) ? 7 * w : 42 + 6 * (w - 6);
  const int nk = (w < 6) ? 7 : 6;
  f32x4 acc[5][4];
#pragma unroll
  for (int a = 0; a < 5; ++a)
#pragma unroll
    for (int b = 0; b < 4; ++b) acc[a][b] = f32x4{0, 0, 0, 0};
  STAMP(4, 0);
  const char* hp = hidF + ((size_t)(4 * bm) * 54 + k_lo) * 1024 + lane * 16;
  const char* wp = W2F + ((size_t)(5 * bn) * 54 + k_lo) * 1024 + lane * 16;
  bf16x8 hb[3][4], wa[3][5];
#pragma unroll
  for (int p = 0; p < 2; ++p) {
#pragma unroll
    for (int i = 0; i < 4; ++i) hb[p][i] = *(const bf16x8*)(hp + (size_t)(i * 54 + p) * 1024);
#pragma unroll
    for (int i = 0; i < 5; ++i) wa[p][i] = *(const bf16x8*)(wp + (size_t)(i * 54 + p) * 1024);
  }
  const int c = lane & 15, g = lane >> 4;
  const int t_lo = (w < 4) ? 3 * w : 12 + 2 * (w - 4);
  const int t_n = (w < 4) ? 3 : 2;
  u32x2 xv[3];
  f32x4 gbv[3];
#pragma unroll
  for (int q = 0; q < 3; ++q) {
    const int tile = (q < t_n) ? t_lo + q : t_lo;
    const int a = tile >> 2, b = tile & 3;
    const int m = 16 * (4 * bm + b) + c;
    const int n = 16 * (5 * bn + a) + 4 * g;
    xv[q] = *(const u32x2*)(x1 + (size_t)m * D_ + n);
    gbv[q] = *(const f32x4*)(modv + 5 * 640 + n);
  }
#pragma unroll
  for (int k = 0; k < 7; ++k) {
    if (k < nk) {
      if (k + 2 < nk) {
#pragma unroll
        for (int i = 0; i < 4; ++i) hb[(k + 2) % 3][i] = *(const bf16x8*)(hp + (size_t)(i * 54 + k + 2) * 1024);
#pragma unroll
        for (int i = 0; i < 5; ++i) wa[(k + 2) % 3][i] = *(const bf16x8*)(wp + (size_t)(i * 54 + k + 2) * 1024);
      }
#pragma unroll
      for (int a = 0; a < 5; ++a)
#pragma unroll
        for (int b = 0; b < 4; ++b)
          acc[a][b] = __builtin_amdgcn_mfma_f32_16x16x32_bf16(wa[k % 3][a], hb[k % 3][b], acc[a][b], 0, 0, 0);
    }
  }
  STAMP(4, 1);
#pragma unroll
  for (int a = 0; a < 5; ++a)
#pragma unroll
    for (int b = 0; b < 4; ++b) *(f32x4*)(sm + ((w * 20 + a * 4 + b) * 64 + lane) * 4) = acc[a][b];
  __syncthreads();
  STAMP(4, 2);
#pragma unroll
  for (int q = 0; q < 3; ++q) {
    if (q < t_n) {
      const int tile = t_lo + q;
      const int a = tile >> 2, b = tile & 3;
      const int m = 16 * (4 * bm + b) + c;
      const int n = 16 * (5 * bn + a) + 4 * g;
      f32x4 s = *(const f32x4*)(sm + ((0 * 20 + tile) * 64 + lane) * 4);
#pragma unroll
      for (int ww = 1; ww < 8; ++ww) s += *(const f32x4*)(sm + ((ww * 20 + tile) * 64 + lane) * 4);
      float r0 = bflo(xv[q][0]) + rbf(gbv[q][0] * rbf(s[0]));
      float r1 = bfhi(xv[q][0]) + rbf(gbv[q][1] * rbf(s[1]));
      float r2 = bflo(xv[q][1]) + rbf(gbv[q][2] * rbf(s[2]));
      float r3 = bfhi(xv[q][1]) + rbf(gbv[q][3] * rbf(s[3]));
      u32x2 ov = {pack2(r0, r1), pack2(r2, r3)};
      *(u32x2*)(out + (size_t)m * D_ + n) = ov;
    }
  }
  STAMP(4, 3);
}

extern "C" void kernel_launch(void* const* d_in, const int* in_sizes, int n_in, void* d_out, int out_size,
                              void* d_ws, size_t ws_size, hipStream_t stream) {
  const u16* x = (const u16*)d_in[0];
  const u16* A_re = (const u16*)d_in[1];
  const u16* A_im = (const u16*)d_in[2];
  const u16* B_re = (const u16*)d_in[3];
  const u16* B_im = (const u16*)d_in[4];
  const u16* C_re = (const u16*)d_in[5];
  const u16* C_im = (const u16*)d_in[6];
  const u16* log_dt = (const u16*)d_in[7];
  const u16* Dskip = (const u16*)d_in[8];
  const u16* w1 = (const u16*)d_in[10];
  const u16* w3 = (const u16*)d_in[11];
  const u16* w2 = (const u16*)d_in[12];
  const u16* cvec = (const u16*)d_in[13];
  const u16* w_mod = (const u16*)d_in[14];
  char* ws = (char*)d_ws;
  u16* out = (u16*)d_out;

  k1_prep<<<K1_KGEN + K1_LN + K1_MOD + K1_RT, 512, 0, stream>>>(x, A_re, A_im, B_re, B_im, C_re, C_im, log_dt,
                                                                 cvec, w_mod, ws);
  k2_conv<<<640 + K2_RT, 512, 0, stream>>>((const u16*)(ws + OFF_XNT), (const float*)(ws + OFF_KD),
                                           (const float*)(ws + OFF_MOD), Dskip, (u16*)(ws + OFF_GYT), w1, w3, w2, ws);
  k3_ln2<<<128, 320, 0, stream>>>((const u16*)(ws + OFF_GYT), x, (const float*)(ws + OFF_MOD), (u16*)(ws + OFF_X1),
                                  ws + OFF_H2F);
  k4_gemm1<<<8 * 28, 512, 0, stream>>>(ws + OFF_H2F, ws + OFF_W13F, ws + OFF_HIDF);
  k5_gemm2<<<32 * 8, 512, 0, stream>>>(ws + OFF_HIDF, ws + OFF_W2F, (const u16*)(ws + OFF_X1),
                                       (const float*)(ws + OFF_MOD), out);
}
```

```cpp
#include <hip/hip_runtime.h>
#include <hip/hip_bf16.h>

typedef __attribute__((ext_vector_type(8))) short bf16x8;
typedef __attribute__((ext_vector_type(4))) float f32x4;
typedef __attribute__((ext_vector_type(4))) unsigned u32x4;
typedef __attribute__((ext_vector_type(2))) unsigned u32x2;
typedef unsigned short u16;

#define DI __device__ __forceinline__

constexpr int L_ = 2048, D_ = 640, N_ = 128, F_ = 1728;

constexpr size_t OFF_MOD = 0;
constexpr size_t OFF_K0 = 16384;
constexpr size_t OFF_XNT = 32768;
constexpr size_t SZ_ACT = (size_t)D_ * L_ * 2;
constexpr int KD_STRIDE = 648;
constexpr size_t OFF_KD = OFF_XNT + SZ_ACT;
constexpr size_t OFF_GYT = OFF_KD + (size_t)D_ * KD_STRIDE * 4;
constexpr size_t OFF_X1 = OFF_GYT + SZ_ACT;
constexpr size_t OFF_H2F = OFF_X1 + SZ_ACT;
constexpr size_t OFF_W13F = OFF_H2F + SZ_ACT;
constexpr size_t SZ_W = (size_t)D_ * F_ * 2;
constexpr size_t OFF_W2F = OFF_W13F + 2 * SZ_W;
constexpr size_t OFF_HIDF = OFF_W2F + SZ_W;

DI float bf2f(unsigned v) { return __uint_as_float(v << 16); }
DI float bflo(unsigned v) { return __uint_as_float(v << 16); }
DI float bfhi(unsigned v) { return __uint_as_float(v & 0xffff0000u); }
DI float rbf(float x) { return (float)(__bf16)x; }
DI unsigned f2bf(float x) { __bf16 b = (__bf16)x; return (unsigned)__builtin_bit_cast(u16, b); }
DI unsigned pack2(float lo, float hi) { return f2bf(lo) | (f2bf(hi) << 16); }
DI float frcp(float x) { return __builtin_amdgcn_rcpf(x); }
DI float fdiv(float a, float b, float rb) { float q = a * rb; float r = fmaf(-q, b, a); return fmaf(r, rb, q); }

DI void transpose8x8(const u32x4* in, u32x4* out) {
#pragma unroll
  for (int d = 0; d < 4; ++d) {
#pragma unroll
    for (int p = 0; p < 4; ++p) {
      unsigned a = in[2 * p][d], b = in[2 * p + 1][d];
      out[2 * d][p] = (a & 0xffffu) | (b << 16);
      out[2 * d + 1][p] = (a >> 16) | (b & 0xffff0000u);
    }
  }
}

typedef __attribute__((ext_vector_type(2))) float c32;
DI c32 cexp_l(float ar, float ai, float l) {
  const float mag = __expf(ar * l);
  float rev = (ai * l) * 0.15915494309189535f;
  rev = rev - floorf(rev);
  c32 cs = {__builtin_amdgcn_cosf(rev), __builtin_amdgcn_sinf(rev)};
  return cs * mag;
}
template <bool FEEDS_MFMA = false>
DI c32 cmul(c32 a, c32 b) {
  c32 t, r;
  asm("v_pk_mul_f32 %0, %1, %2 op_sel_hi:[0,1]" : "=v"(t) : "v"(a), "v"(b));
  if constexpr (FEEDS_MFMA)
    asm("v_pk_fma_f32 %0, %1, %2, %3 op_sel:[1,1,0] op_sel_hi:[1,0,1] neg_lo:[0,1,0]\n\ts_nop 1"
        : "=v"(r) : "v"(a), "v"(b), "v"(t));
  else
    asm("v_pk_fma_f32 %0, %1, %2, %3 op_sel:[1,1,0] op_sel_hi:[1,0,1] neg_lo:[0,1,0]"
        : "=v"(r) : "v"(a), "v"(b), "v"(t));
  return r;
}

DI void retile_task(int task, const u16* __restrict__ w1, const u16* __restrict__ w3, const u16* __restrict__ w2,
                    char* __restrict__ ws) {
  if (task >= 3 * 17280) return;
  const int which = task / 17280, tk = task % 17280;
  u32x4 v[8], o[8];
  if (which < 2) {
    const int nc = tk % 216, kc = tk / 216;
    const u16* src = which == 0 ? w1 : w3;
#pragma unroll
    for (int i = 0; i < 8; ++i) v[i] = *(const u32x4*)(src + (size_t)(8 * kc + i) * F_ + 8 * nc);
    transpose8x8(v, o);
    const int nt = nc >> 1, kt = kc >> 2, g = kc & 3;
    char* dst = ws + OFF_W13F + ((size_t)((nt * 20 + kt) * 2 + which)) * 1024 + (size_t)(16 * g + 8 * (nc & 1)) * 16;
#pragma unroll
    for (int c = 0; c < 8; ++c) *(u32x4*)(dst + c * 16) = o[c];
  } else {
    const int nc = tk % 80, kc = tk / 80;
#pragma unroll
    for (int i = 0; i < 8; ++i) v[i] = *(const u32x4*)(w2 + (size_t)(8 * kc + i) * D_ + 8 * nc);
    transpose8x8(v, o);
    const int nt = nc >> 1, kt = kc >> 2, g = kc & 3;
    char* dst = ws + OFF_W2F + ((size_t)(nt * 54 + kt)) * 1024 + (size_t)(16 * g + 8 * (nc & 1)) * 16;
#pragma unroll
    for (int c = 0; c < 8; ++c) *(u32x4*)(dst + c * 16) = o[c];
  }
}

constexpr int K1_KGEN = 640, K1_LN = 256, K1_MOD = 120, K1_RT = 0, K2_RT = 102;

__global__ __launch_bounds__(512) void k1_prep(
    const u16* __restrict__ x, const u16* __restrict__ A_re, const u16* __restrict__ A_im,
    const u16* __restrict__ B_re, const u16* __restrict__ B_im, const u16* __restrict__ C_re,
    const u16* __restrict__ C_im, const u16* __restrict__ log_dt, const u16* __restrict__ cvec,
    const u16* __restrict__ w_mod, char* __restrict__ ws) {
  __shared__ __attribute__((aligned(16))) float sm[8 * 4 * 256 + 1536];
  const int t = threadIdx.x;
  int bid = blockIdx.x;
  if (bid < K1_KGEN) {
    const int d = bid;
    const int w = t >> 6, lane = t & 63, i = lane & 15, kk = lane >> 4;
    float* prm = sm + 8192;
    if (t < 128) {
      const int n = d * N_ + t;
      const float dt = rbf(__expf(bf2f(log_dt[d])));
      const float ar = dt * bf2f(A_re[n]), ai = dt * bf2f(A_im[n]);
      const c32 db = {dt * bf2f(B_re[n]), dt * bf2f(B_im[n])};
      const c32 cc = {bf2f(C_re[n]), bf2f(C_im[n])};
      const c32 c0 = cmul(cc, db);
      c32 gg = cexp_l(ar, ai, 256.f);
      const c32 c1 = cmul(gg, c0);
      gg = cmul(gg, gg);
      const c32 c2 = cmul(gg, c0);
      gg = cmul(gg, gg);
      const c32 c3 = cmul(gg, c0);
      *(f32x4*)(prm + t * 12) = f32x4{ar, ai, c0.x, c0.y};
      *(f32x4*)(prm + t * 12 + 4) = f32x4{c1.x, c1.y, c2.x, c2.y};
      *(f32x4*)(prm + t * 12 + 8) = f32x4{c3.x, c3.y, 0.f, 0.f};
    }
    __syncthreads();
    f32x4 acc0 = {0, 0, 0, 0}, acc1 = {0, 0, 0, 0}, acc2 = {0, 0, 0, 0}, acc3 = {0, 0, 0, 0};
    const float fi = (float)i;
#pragma unroll
    for (int s = 0; s < 4; ++s) {
      const int n = 16 * w + 4 * s + kk;
      const f32x4 p0 = *(const f32x4*)(prm + n * 12), p1 = *(const f32x4*)(prm + n * 12 + 4), p2 = *(const f32x4*)(prm + n * 12 + 8);
      c32 e = cexp_l(p0[0], p0[1], fi);
      c32 xx = cexp_l(p0[0], p0[1], 16.f * fi);
      c32 pp;
      pp = cmul<true>(xx, c32{p0[2], p0[3]});
      acc0 = __builtin_amdgcn_mfma_f32_16x16x4f32(pp.x, e.x, acc0, 0, 0, 0);
      acc0 = __builtin_amdgcn_mfma_f32_16x16x4f32(-pp.y, e.y, acc0, 0, 0, 0);
      e = cmul<true>(e, e); xx = cmul(xx, xx);
      pp = cmul<true>(xx, c32{p1[0], p1[1]});
      acc1 = __builtin_amdgcn_mfma_f32_16x16x4f32(pp.x, e.x, acc1, 0, 0, 0);
      acc1 = __builtin_amdgcn_mfma_f32_16x16x4f32(-pp.y, e.y, acc1, 0, 0, 0);
      e = cmul<true>(e, e); xx = cmul(xx, xx);
      pp = cmul<true>(xx, c32{p1[2], p1[3]});
      acc2 = __builtin_amdgcn_mfma_f32_16x16x4f32(pp.x, e.x, acc2, 0, 0, 0);
      acc2 = __builtin_amdgcn_mfma_f32_16x16x4f32(-pp.y, e.y, acc2, 0, 0, 0);
      e = cmul<true>(e, e); xx = cmul(xx, xx);
      pp = cmul<true>(xx, c32{p2[0], p2[1]});
      acc3 = __builtin_amdgcn_mfma_f32_16x16x4f32(pp.x, e.x, acc3, 0, 0, 0);
      acc3 = __builtin_amdgcn_mfma_f32_16x16x4f32(-pp.y, e.y, acc3, 0, 0, 0);
    }
    float* red = sm;
#pragma unroll
    for (int j = 0; j < 4; ++j) {
      const int idx = 16 * (4 * kk + j) + i;
      red[(w * 4 + 0) * 256 + idx] = acc0[j];
      red[(w * 4 + 1) * 256 + idx] = acc1[j];
      red[(w * 4 + 2) * 256 + idx] = acc2[j];
      red[(w * 4 + 3) * 256 + idx] = acc3[j];
    }
    __syncthreads();
    float* kd = (float*)(ws + OFF_KD) + (size_t)d * KD_STRIDE;
    for (int ci = t; ci < 641; ci += 512) {
      int tile, idx;
      if (ci < 256) { tile = 0; idx = ci; }
      else if (ci < 384) { tile = 1; idx = ci - 256; }
      else if (ci < 512) { tile = 2; idx = ci - 384; }
      else { tile = 3; idx = ci - 512; }
      float v = 0.f;
#pragma unroll
      for (int ww = 0; ww < 8; ++ww) v += red[(ww * 4 + tile) * 256 + idx];
      kd[ci] = v;
    }
    return;
  }
  bid -= K1_KGEN;
  if (bid < K1_LN) {
    const int l0 = ((bid & 7) * 32 + (bid >> 3)) * 8;
    const int w = t >> 6, lane = t & 63;
    const unsigned* xr = (const unsigned*)(x + (size_t)(l0 + w) * D_);
    unsigned v[5];
#pragma unroll
    for (int j = 0; j < 5; ++j) v[j] = xr[lane + 64 * j];
    float s = 0.f;
#pragma unroll
    for (int j = 0; j < 5; ++j) s += bflo(v[j]) + bfhi(v[j]);
#pragma unroll
    for (int o = 1; o < 64; o <<= 1) s += __shfl_xor(s, o);
    const float mean = s * (1.f / 640.f);
    float q = 0.f;
#pragma unroll
    for (int j = 0; j < 5; ++j) {
      float a = bflo(v[j]) - mean, b = bfhi(v[j]) - mean;
      q += a * a + b * b;
    }
#pragma unroll
    for (int o = 1; o < 64; o <<= 1) q += __shfl_xor(q, o);
    const float mb = rbf(mean);
    const float den = rbf(sqrtf(rbf(rbf(q * (1.f / 640.f)) + 1e-5f)));
    const float rden = frcp(den);
    unsigned* sn = (unsigned*)sm;
#pragma unroll
    for (int j = 0; j < 5; ++j) {
      float a = fdiv(rbf(bflo(v[j]) - mb), den, rden), b = fdiv(rbf(bfhi(v[j]) - mb), den, rden);
      sn[w * 320 + lane + 64 * j] = pack2(a, b);
    }
    __syncthreads();
    const u16* sn16 = (const u16*)sn;
    u16* xnT = (u16*)(ws + OFF_XNT);
    for (int dd = t; dd < 640; dd += 512) {
      u32x4 a;
#pragma unroll
      for (int e = 0; e < 4; ++e)
        a[e] = (unsigned)sn16[(2 * e) * 640 + dd] | ((unsigned)sn16[(2 * e + 1) * 640 + dd] << 16);
      *(u32x4*)(xnT + (size_t)dd * L_ + l0) = a;
    }
    return;
  }
  bid -= K1_LN;
  if (bid < K1_MOD) {
    const int j0 = bid * 32;
    const int cc = t & 3, rgp = t >> 2;
    u32x4 wv[5];
    u16 cv[5];
#pragma unroll
    for (int r = 0; r < 5; ++r) {
      const int i = rgp * 5 + r;
      wv[r] = *(const u32x4*)(w_mod + (size_t)i * 3840 + j0 + 8 * cc);
      cv[r] = cvec[i];
    }
    float acc[8];
#pragma unroll
    for (int e = 0; e < 8; ++e) acc[e] = 0.f;
#pragma unroll
    for (int r = 0; r < 5; ++r) {
      const float c_ = bf2f(cv[r]);
      const float sv = rbf(c_ / (1.f + __expf(-c_)));
#pragma unroll
      for (int e = 0; e < 4; ++e) {
        acc[2 * e] += sv * bflo(wv[r][e]);
        acc[2 * e + 1] += sv * bfhi(wv[r][e]);
      }
    }
    float* red = sm;
#pragma unroll
    for (int e = 0; e < 8; ++e) red[rgp * 33 + 8 * cc + e] = acc[e];
    __syncthreads();
    {
      const int col = t >> 4, part = t & 15;
      float s = 0.f;
#pragma unroll
      for (int r = 0; r < 8; ++r) s += red[(part * 8 + r) * 33 + col];
#pragma unroll
      for (int o = 1; o < 16; o <<= 1) s += __shfl_xor(s, o);
      if (part == 0) ((float*)(ws + OFF_MOD))[j0 + col] = rbf(s);
    }
    return;
  }
}

constexpr int UC_STRIDE = 4608;
constexpr int UC_BYTES = 8 * UC_STRIDE;
constexpr int KR_BYTES = 384 * 16;
constexpr int K2_LDS = UC_BYTES + 2 * KR_BYTES;

DI int lag_to_ci(int lag) {
  const int r = (int)rbf((float)lag);
  if (r < 256) return r;
  if (r < 512) return 256 + ((r - 256) >> 1);
  if (r < 1024) return 384 + ((r - 512) >> 2);
  return 512 + ((r - 1024) >> 3);
}

__global__ __launch_bounds__(512) void k2_conv(const u16* __restrict__ xnT, const float* __restrict__ KD,
                                               const float* __restrict__ modv, const u16* __restrict__ Dskip,
                                               u16* __restrict__ gyT, const u16* __restrict__ w1,
                                               const u16* __restrict__ w3, const u16* __restrict__ w2,
                                               char* __restrict__ ws) {
  __shared__ __attribute__((aligned(16))) char sm[K2_LDS];
  if (blockIdx.x >= 640) { retile_task((blockIdx.x - 640) * 512 + threadIdx.x, w1, w3, w2, ws); return; }
  char* uc = sm;
  char* krh = sm + UC_BYTES;
  char* krl = krh + KR_BYTES;
  const int d = blockIdx.x, t = threadIdx.x;
  const int w = t >> 6, lane = t & 63, c = lane & 15, g = lane >> 4;
  const float* kd = KD + (size_t)d * KD_STRIDE;
  const float sa = modv[d], ca1 = rbf(1.f + modv[640 + d]), ga = modv[1280 + d];
  const float dsk = bf2f(Dskip[d]), k0 = kd[0];
  const u32x2 xn_own = ((const u32x2*)(xnT + (size_t)d * L_))[t];

  if (t < 256) {
    const u32x4* xn4 = (const u32x4*)(xnT + (size_t)d * L_);
    u32x4 cur = xn4[t];
    u32x4 prv = {0, 0, 0, 0};
    if (t > 0) prv = xn4[t - 1];
    unsigned hv[8];
#pragma unroll
    for (int e = 0; e < 4; ++e) {
      float a = rbf(rbf(bflo(prv[e]) * ca1) + sa), b = rbf(rbf(bfhi(prv[e]) * ca1) + sa);
      hv[e] = (t > 0) ? pack2(a, b) : 0u;
      float a2 = rbf(rbf(bflo(cur[e]) * ca1) + sa), b2 = rbf(rbf(bfhi(cur[e]) * ca1) + sa);
      hv[4 + e] = pack2(a2, b2);
    }
#pragma unroll
    for (int s = 0; s < 8; ++s) {
      u32x4 o;
      if ((s & 1) == 0) {
#pragma unroll
        for (int e = 0; e < 4; ++e) o[e] = hv[s / 2 + e];
      } else {
#pragma unroll
        for (int e = 0; e < 4; ++e) o[e] = (hv[s / 2 + e] >> 16) | (hv[s / 2 + e + 1] << 16);
      }
      *(u32x4*)(uc + s * UC_STRIDE + 64 * (s & 3) + 32 * (s >> 2) + 16 * (t + 7)) = o;
    }
  } else {
    const int t2 = t - 256;
    float kv[8];
#pragma unroll
    for (int u = 0; u < 8; ++u) {
      const int lag = 2048 - (8 * t2 + u);
      kv[u] = kd[lag_to_ci(lag < 2048 ? lag : 2047)];
      if (lag == 2048) kv[u] = 0.f;
    }
    if (t2 < 56) {
      const int s = t2 / 7, m = t2 % 7;
      *(u32x4*)(uc + s * UC_STRIDE + 64 * (s & 3) + 32 * (s >> 2) + 16 * m) = u32x4{0, 0, 0, 0};
    }
    if (t2 < 120) {
      *(u32x4*)(krh + 16 * (264 + t2)) = u32x4{0, 0, 0, 0};
      *(u32x4*)(krl + 16 * (264 + t2)) = u32x4{0, 0, 0, 0};
    }
    u32x4 kh, kl;
#pragma unroll
    for (int e = 0; e < 4; ++e) {
      const float h0 = rbf(kv[2 * e]), h1 = rbf(kv[2 * e + 1]);
      kh[e] = pack2(h0, h1);
      kl[e] = pack2(kv[2 * e] - h0, kv[2 * e + 1] - h1);
    }
    const int ci = 8 + t2, R = ci >> 3, e = ci & 7;
    const int pos = 8 * R + (e ^ (R & 7));
    *(u32x4*)(krh + 16 * pos) = kh;
    *(u32x4*)(krl + 16 * pos) = kl;
  }
  __syncthreads();

  f32x4 acc[2][4];
#pragma unroll
  for (int a = 0; a < 2; ++a)
#pragma unroll
    for (int b = 0; b < 4; ++b) acc[a][b] = f32x4{0, 0, 0, 0};

  const int sg = c & 7;
  const char* ub = uc + sg * UC_STRIDE + 64 * (sg & 3) + 32 * (sg >> 2) + 16 * ((c >> 3) + g);
  int dA_lo, dA_hi, s_lo, s_hi;
  if (w < 5) { dA_lo = 6 * w; dA_hi = 6 * w + 6; s_lo = 32; s_hi = 32; }
  else if (w == 5) { dA_lo = 30; dA_hi = 32; s_lo = 32; s_hi = 40; }
  else { dA_lo = 32; dA_hi = 32; s_lo = 40 + 12 * (w - 6); s_hi = s_lo + 12; }
  for (int kp = dA_lo; kp < dA_hi; ++kp) {
    bf16x8 bfr[4];
#pragma unroll
    for (int ct = 0; ct < 4; ++ct) bfr[ct] = *(const bf16x8*)(ub + 16 * (2 * ct + 4 * kp));
    const int e = 4 * (kp & 1) + g;
    bf16x8 ah[2], al[2];
#pragma unroll
    for (int tt = 0; tt < 2; ++tt) {
      const int R = 32 - 16 * tt - c + (kp >> 1);
      const int pos = 8 * R + (e ^ (R & 7));
      ah[tt] = *(const bf16x8*)(krh + 16 * pos);
      al[tt] = *(const bf16x8*)(krl + 16 * pos);
    }
#pragma unroll
    for (int tt = 0; tt < 2; ++tt)
#pragma unroll
      for (int ct = 0; ct < 4; ++ct) {
        acc[tt][ct] = __builtin_amdgcn_mfma_f32_16x16x32_bf16(ah[tt], bfr[ct], acc[tt][ct], 0, 0, 0);
        acc[tt][ct] = __builtin_amdgcn_mfma_f32_16x16x32_bf16(al[tt], bfr[ct], acc[tt][ct], 0, 0, 0);
      }
  }
  for (int kp = s_lo; kp < s_hi; ++kp) {
    bf16x8 bfr[4];
#pragma unroll
    for (int ct = 0; ct < 4; ++ct) bfr[ct] = *(const bf16x8*)(ub + 16 * (2 * ct + 4 * kp));
    const int e = 4 * (kp & 1) + g;
    const int R = 16 - c + (kp >> 1);
    const int pos = 8 * R + (e ^ (R & 7));
    const bf16x8 ah = *(const bf16x8*)(krh + 16 * pos);
    const bf16x8 al = *(const bf16x8*)(krl + 16 * pos);
#pragma unroll
    for (int ct = 0; ct < 4; ++ct) {
      acc[1][ct] = __builtin_amdgcn_mfma_f32_16x16x32_bf16(ah, bfr[ct], acc[1][ct], 0, 0, 0);
      acc[1][ct] = __builtin_amdgcn_mfma_f32_16x16x32_bf16(al, bfr[ct], acc[1][ct], 0, 0, 0);
    }
  }
  __syncthreads();
  float* yb_ = (float*)sm;
  if (w >= 4) {
#pragma unroll
    for (int tt = 0; tt < 2; ++tt)
#pragma unroll
      for (int ct = 0; ct < 4; ++ct) *(f32x4*)(yb_ + (((w - 4) * 8 + tt * 4 + ct) * 64 + lane) * 4) = acc[tt][ct];
  }
  __syncthreads();
  if (w < 4) {
#pragma unroll
    for (int tt = 0; tt < 2; ++tt)
#pragma unroll
      for (int ct = 0; ct < 4; ++ct) acc[tt][ct] += *(const f32x4*)(yb_ + ((w * 8 + tt * 4 + ct) * 64 + lane) * 4);
  }
  __syncthreads();
  if (w < 4) {
    float* yp = yb_ + w * 2048;
#pragma unroll
    for (int tt = 0; tt < 2; ++tt)
#pragma unroll
      for (int ct = 0; ct < 4; ++ct)
#pragma unroll
        for (int j = 0; j < 4; ++j) yp[64 * (16 * tt + 4 * g + j) + 16 * ct + c] = acc[tt][ct][j];
  }
  __syncthreads();
  const float* y0 = yb_ + 4 * t;
  f32x4 s0 = *(const f32x4*)(y0);
#pragma unroll
  for (int ww = 1; ww < 4; ++ww) s0 += *(const f32x4*)(y0 + ww * 2048);
  float r[4];
#pragma unroll
  for (int i = 0; i < 4; ++i) {
    const float xn = (i & 1) ? bfhi(xn_own[i >> 1]) : bflo(xn_own[i >> 1]);
    const float hh = rbf(rbf(xn * ca1) + sa);
    const float yb = rbf(s0[i] + k0 * hh);
    const float sk = rbf(hh * dsk);
    const float sy = rbf(yb + sk);
    r[i] = ga * sy;
  }
  ((u32x2*)(gyT + (size_t)d * L_))[t] = u32x2{pack2(r[0], r[1]), pack2(r[2], r[3])};
}

__global__ __launch_bounds__(320) void k3_ln2(const u16* __restrict__ gyT, const u16* __restrict__ x,
                                              const float* __restrict__ modv, u16* __restrict__ x1,
                                              char* __restrict__ h2F) {
  __shared__ float red[16 * 81];
  __shared__ float stat[32];
  const int t = threadIdx.x;
  const int l0 = ((blockIdx.x & 7) * 16 + (blockIdx.x >> 3)) * 16;
  const int cg = t & 3, ch = t >> 2;
  u32x2 v[8];
#pragma unroll
  for (int i = 0; i < 8; ++i) v[i] = *(const u32x2*)(gyT + (size_t)(8 * ch + i) * L_ + l0 + 4 * cg);
  u32x4 xr[4];
#pragma unroll
  for (int c = 0; c < 4; ++c) xr[c] = *(const u32x4*)(x + (size_t)(l0 + 4 * cg + c) * D_ + 8 * ch);
  const f32x4 sb0 = *(const f32x4*)(modv + 3 * 640 + 8 * ch), sb1 = *(const f32x4*)(modv + 3 * 640 + 8 * ch + 4);
  const f32x4 cb0 = *(const f32x4*)(modv + 4 * 640 + 8 * ch), cb1 = *(const f32x4*)(modv + 4 * 640 + 8 * ch + 4);
  u32x4 o[4];
#pragma unroll
  for (int dd = 0; dd < 2; ++dd)
#pragma unroll
    for (int p = 0; p < 4; ++p) {
      unsigned a = v[2 * p][dd], b = v[2 * p + 1][dd];
      o[2 * dd][p] = pack2(bflo(xr[2 * dd][p]) + bflo(a), bfhi(xr[2 * dd][p]) + bflo(b));
      o[2 * dd + 1][p] = pack2(bflo(xr[2 * dd + 1][p]) + bfhi(a), bfhi(xr[2 * dd + 1][p]) + bfhi(b));
    }
#pragma unroll
  for (int c = 0; c < 4; ++c) {
    float s = 0.f;
#pragma unroll
    for (int e = 0; e < 4; ++e) s += bflo(o[c][e]) + bfhi(o[c][e]);
    red[(4 * cg + c) * 81 + ch] = s;
  }
  __syncthreads();
  if (t < 256) {
    const int row = t >> 4, part = t & 15;
    float s = 0.f;
#pragma unroll
    for (int e = 0; e < 5; ++e) s += red[row * 81 + part * 5 + e];
#pragma unroll
    for (int of = 1; of < 16; of <<= 1) s += __shfl_xor(s, of);
    if (part == 0) stat[row] = s * (1.f / 640.f);
  }
  __syncthreads();
  float mean[4];
#pragma unroll
  for (int c = 0; c < 4; ++c) {
    mean[c] = stat[4 * cg + c];
    float s = 0.f;
#pragma unroll
    for (int e = 0; e < 4; ++e) {
      float a = bflo(o[c][e]) - mean[c], b = bfhi(o[c][e]) - mean[c];
      s += a * a + b * b;
    }
    red[(4 * cg + c) * 81 + ch] = s;
  }
  __syncthreads();
  if (t < 256) {
    const int row = t >> 4, part = t & 15;
    float s = 0.f;
#pragma unroll
    for (int e = 0; e < 5; ++e) s += red[row * 81 + part * 5 + e];
#pragma unroll
    for (int of = 1; of < 16; of <<= 1) s += __shfl_xor(s, of);
    if (part == 0) stat[16 + row] = s * (1.f / 640.f);
  }
  __syncthreads();
  float sbv[8] = {sb0[0], sb0[1], sb0[2], sb0[3], sb1[0], sb1[1], sb1[2], sb1[3]};
  float cbv[8] = {cb0[0], cb0[1], cb0[2], cb0[3], cb1[0], cb1[1], cb1[2], cb1[3]};
#pragma unroll
  for (int i = 0; i < 8; ++i) cbv[i] = rbf(1.f + cbv[i]);
  const int kt = ch >> 2, gq = ch & 3;
#pragma unroll
  for (int c = 0; c < 4; ++c) {
    const int m = l0 + 4 * cg + c;
    const float mb = rbf(mean[c]);
    const float den = rbf(sqrtf(rbf(rbf(stat[16 + 4 * cg + c]) + 1e-5f)));
    const float rden = frcp(den);
    u32x4 hv;
#pragma unroll
    for (int e = 0; e < 4; ++e) {
      float a = rbf(fdiv(rbf(bflo(o[c][e]) - mb), den, rden)), b = rbf(fdiv(rbf(bfhi(o[c][e]) - mb), den, rden));
      a = rbf(a * cbv[2 * e]) + sbv[2 * e];
      b = rbf(b * cbv[2 * e + 1]) + sbv[2 * e + 1];
      hv[e] = pack2(a, b);
    }
    *(u32x4*)(h2F + ((size_t)((m >> 4) * 20 + kt)) * 1024 + (size_t)((m & 15) + 16 * gq) * 16) = hv;
    *(u32x4*)(x1 + (size_t)m * D_ + 8 * ch) = o[c];
  }
}

constexpr int G1_SLOT = 8192, G1_PF = 4;

__global__ __launch_bounds__(512) void k4_gemm1(const char* __restrict__ h2F, const char* __restrict__ W13F,
                                                char* __restrict__ hidF) {
  __shared__ __attribute__((aligned(16))) char sm[3 * G1_SLOT];
  const int t = threadIdx.x, w = t >> 6, lane = t & 63;
  const int xcd = blockIdx.x & 7, idx = blockIdx.x >> 3;
  const int nq = xcd >> 1, nstart = 7 * nq, ncnt = (nq == 3) ? 6 : 7;
  if (idx >= 4 * ncnt) return;
  const int bm = 4 * (xcd & 1) + (idx & 3), bn = nstart + (idx >> 2);
  const char* hsrc = h2F + ((size_t)((16 * bm + 2 * w) * 20)) * 1024 + lane * 16;
  const char* wsrc = W13F + ((size_t)((4 * bn + (w >> 1)) * 20)) * 2048 + (w & 1) * 1024 + lane * 16;
  f32x4 acc[4][2][2];
#pragma unroll
  for (int a = 0; a < 4; ++a)
#pragma unroll
    for (int b = 0; b < 2; ++b)
#pragma unroll
      for (int cI = 0; cI < 2; ++cI) acc[a][b][cI] = f32x4{0, 0, 0, 0};
  bf16x8 hf[G1_PF][2];
  u32x4 pw[G1_PF];
#define G1_LOADH(R, S) do { hf[R][0] = *(const bf16x8*)(hsrc + (size_t)(S) * 1024); \
    hf[R][1] = *(const bf16x8*)(hsrc + (size_t)(S) * 1024 + 20 * 1024); } while (0)
#define G1_LOADW(R, S) do { pw[R] = *(const u32x4*)(wsrc + (size_t)(S) * 2048); } while (0)
#define G1_WRITE(R, S) do { *(u32x4*)(sm + ((S) % 3) * G1_SLOT + w * 1024 + lane * 16) = pw[R]; } while (0)
#define G1_READ(B, S) do { const char* b_ = sm + ((S) % 3) * G1_SLOT; \
    _Pragma("unroll") for (int a = 0; a < 4; ++a) _Pragma("unroll") for (int b = 0; b < 2; ++b) \
      wa[B][a][b] = *(const bf16x8*)(b_ + (a * 2 + b) * 1024 + lane * 16); } while (0)
  bf16x8 wa[2][4][2];
#pragma unroll
  for (int s = 0; s < G1_PF; ++s) { G1_LOADW(s, s); G1_LOADH(s, s); }
  G1_WRITE(0, 0); G1_LOADW(0, G1_PF);
  G1_WRITE(1, 1); G1_LOADW(1, G1_PF + 1);
  __syncthreads();
  G1_READ(0, 0);
#pragma unroll
  for (int s = 0; s < 20; ++s) {
    if (s + 2 < 20) {
      G1_WRITE((s + 2) % G1_PF, s + 2);
      if (s + 2 + G1_PF < 20) G1_LOADW((s + 2) % G1_PF, s + 2 + G1_PF);
    }
    if (s + 1 < 20) G1_READ((s + 1) & 1, s + 1);
    __builtin_amdgcn_sched_barrier(0);
    __builtin_amdgcn_s_setprio(1);
#pragma unroll
    for (int a = 0; a < 4; ++a)
#pragma unroll
      for (int b = 0; b < 2; ++b)
#pragma unroll
        for (int i = 0; i < 2; ++i)
          acc[a][b][i] = __builtin_amdgcn_mfma_f32_16x16x32_bf16(wa[s & 1][a][b], hf[s % G1_PF][i], acc[a][b][i], 0, 0, 0);
    __builtin_amdgcn_s_setprio(0);
    if (s + G1_PF < 20) G1_LOADH(s % G1_PF, s + G1_PF);
    __builtin_amdgcn_sched_barrier(0);
    if (s + 2 < 20) __syncthreads();
  }
  const int cl = lane & 15, g = lane >> 4;
#pragma unroll
  for (int a = 0; a < 4; ++a) {
    const int nt = 4 * bn + a;
    const int kt2 = nt >> 1;
    const int slot = 16 * (2 * (nt & 1) + (g >> 1)) + cl;
#pragma unroll
    for (int i = 0; i < 2; ++i) {
      const int mt = 16 * bm + 2 * w + i;
      float hv[4];
#pragma unroll
      for (int j = 0; j < 4; ++j) {
        const float gv = rbf(acc[a][0][i][j]), uv = rbf(acc[a][1][i][j]);
        const float dn = 1.f + __expf(-gv);
        const float sv = rbf(fdiv(gv, dn, frcp(dn)));
        hv[j] = sv * uv;
      }
      u32x2 pv = {pack2(hv[0], hv[1]), pack2(hv[2], hv[3])};
      *(u32x2*)(hidF + ((size_t)(mt * 54 + kt2)) * 1024 + slot * 16 + (g & 1) * 8) = pv;
    }
  }
}

__global__ __launch_bounds__(512) void k5_gemm2(const char* __restrict__ hidF, const char* __restrict__ W2F,
                                                const u16* __restrict__ x1, const float* __restrict__ modv,
                                                u16* __restrict__ out) {
  __shared__ __attribute__((aligned(16))) float sm[8 * 20 * 256];
  const int t = threadIdx.x, w = t >> 6, lane = t & 63;
  const int bn = (blockIdx.x >> 3) & 7, bm = 4 * (blockIdx.x & 7) + (blockIdx.x >> 6);
  const int k_lo = (w < 6) ? 7 * w : 42 + 6 * (w - 6);
  const int nk = (w < 6) ? 7 : 6;
  f32x4 acc[5][4];
#pragma unroll
  for (int a = 0; a < 5; ++a)
#pragma unroll
    for (int b = 0; b < 4; ++b) acc[a][b] = f32x4{0, 0, 0, 0};
  const char* hp = hidF + ((size_t)(4 * bm) * 54 + k_lo) * 1024 + lane * 16;
  const char* wp = W2F + ((size_t)(5 * bn) * 54 + k_lo) * 1024 + lane * 16;
  bf16x8 hb[3][4], wa[3][5];
#pragma unroll
  for (int p = 0; p < 2; ++p) {
#pragma unroll
    for (int i = 0; i < 4; ++i) hb[p][i] = *(const bf16x8*)(hp + (size_t)(i * 54 + p) * 1024);
#pragma unroll
    for (int i = 0; i < 5; ++i) wa[p][i] = *(const bf16x8*)(wp + (size_t)(i * 54 + p) * 1024);
  }
  const int c = lane & 15, g = lane >> 4;
  const int t_lo = (w < 4) ? 3 * w : 12 + 2 * (w - 4);
  const int t_n = (w < 4) ? 3 : 2;
  u32x2 xv[3];
  f32x4 gbv[3];
#pragma unroll
  for (int q = 0; q < 3; ++q) {
    const int tile = (q < t_n) ? t_lo + q : t_lo;
    const int a = tile >> 2, b = tile & 3;
    const int m = 16 * (4 * bm + b) + c;
    const int n = 16 * (5 * bn + a) + 4 * g;
    xv[q] = *(const u32x2*)(x1 + (size_t)m * D_ + n);
    gbv[q] = *(const f32x4*)(modv + 5 * 640 + n);
  }
#pragma unroll
  for (int k = 0; k < 7; ++k) {
    if (k < nk) {
      if (k + 2 < nk) {
#pragma unroll
        for (int i = 0; i < 4; ++i) hb[(k + 2) % 3][i] = *(const bf16x8*)(hp + (size_t)(i * 54 + k + 2) * 1024);
#pragma unroll
        for (int i = 0; i < 5; ++i) wa[(k + 2) % 3][i] = *(const bf16x8*)(wp + (size_t)(i * 54 + k + 2) * 1024);
      }
#pragma unroll
      for (int a = 0; a < 5; ++a)
#pragma unroll
        for (int b = 0; b < 4; ++b)
          acc[a][b] = __builtin_amdgcn_mfma_f32_16x16x32_bf16(wa[k % 3][a], hb[k % 3][b], acc[a][b], 0, 0, 0);
    }
  }
#pragma unroll
  for (int a = 0; a < 5; ++a)
#pragma unroll
    for (int b = 0; b < 4; ++b) *(f32x4*)(sm + ((w * 20 + a * 4 + b) * 64 + lane) * 4) = acc[a][b];
  __syncthreads();
#pragma unroll
  for (int q = 0; q < 3; ++q) {
    if (q < t_n) {
      const int tile = t_lo + q;
      const int a = tile >> 2, b = tile & 3;
      const int m = 16 * (4 * bm + b) + c;
      const int n = 16 * (5 * bn + a) + 4 * g;
      f32x4 s = *(const f32x4*)(sm + ((0 * 20 + tile) * 64 + lane) * 4);
#pragma unroll
      for (int ww = 1; ww < 8; ++ww) s += *(const f32x4*)(sm + ((ww * 20 + tile) * 64 + lane) * 4);
      float r0 = bflo(xv[q][0]) + rbf(gbv[q][0] * rbf(s[0]));
      float r1 = bfhi(xv[q][0]) + rbf(gbv[q][1] * rbf(s[1]));
      float r2 = bflo(xv[q][1]) + rbf(gbv[q][2] * rbf(s[2]));
      float r3 = bfhi(xv[q][1]) + rbf(gbv[q][3] * rbf(s[3]));
      u32x2 ov = {pack2(r0, r1), pack2(r2, r3)};
      *(u32x2*)(out + (size_t)m * D_ + n) = ov;
    }
  }
}

extern "C" void kernel_launch(void* const* d_in, const int* in_sizes, int n_in, void* d_out, int out_size,
                              void* d_ws, size_t ws_size, hipStream_t stream) {
  const u16* x = (const u16*)d_in[0];
  const u16* A_re = (const u16*)d_in[1];
  const u16* A_im = (const u16*)d_in[2];
  const u16* B_re = (const u16*)d_in[3];
  const u16* B_im = (const u16*)d_in[4];
  const u16* C_re = (const u16*)d_in[5];
  const u16* C_im = (const u16*)d_in[6];
  const u16* log_dt = (const u16*)d_in[7];
  const u16* Dskip = (const u16*)d_in[8];
  const u16* w1 = (const u16*)d_in[10];
  const u16* w3 = (const u16*)d_in[11];
  const u16* w2 = (const u16*)d_in[12];
  const u16* cvec = (const u16*)d_in[13];
  const u16* w_mod = (const u16*)d_in[14];
  char* ws = (char*)d_ws;
  u16* out = (u16*)d_out;

  k1_prep<<<K1_KGEN + K1_LN + K1_MOD + K1_RT, 512, 0, stream>>>(x, A_re, A_im, B_re, B_im, C_re, C_im, log_dt,
                                                                 cvec, w_mod, ws);
  k2_conv<<<640 + K2_RT, 512, 0, stream>>>((const u16*)(ws + OFF_XNT), (const float*)(ws + OFF_KD),
                                           (const float*)(ws + OFF_MOD), Dskip, (u16*)(ws + OFF_GYT), w1, w3, w2, ws);
  k3_ln2<<<128, 320, 0, stream>>>((const u16*)(ws + OFF_GYT), x, (const float*)(ws + OFF_MOD), (u16*)(ws + OFF_X1),
                                  ws + OFF_H2F);
  k4_gemm1<<<8 * 28, 512, 0, stream>>>(ws + OFF_H2F, ws + OFF_W13F, ws + OFF_HIDF);
  k5_gemm2<<<32 * 8, 512, 0, stream>>>(ws + OFF_HIDF, ws + OFF_W2F, (const u16*)(ws + OFF_X1),
                                       (const float*)(ws + OFF_MOD), out);
}
```
